```python
import torch

def _softmax(x, axis=-1):
    return torch.softmax(x, dim=axis)
def _gelu(x):
    return torch.nn.functional.gelu(x, approximate="tanh")
def _silu(x):
    return torch.nn.functional.silu(x)
def _sigmoid(x):
    return torch.sigmoid(x)
def _layernorm(x, axis=-1, eps=1e-5):
    m = x.mean(dim=axis, keepdim=True)
    v = x.var(dim=axis, keepdim=True, unbiased=False)
    return (x - m) / torch.sqrt(v + eps)
def _scatter_add(tgt, idx, src, axis=0):
    return tgt.clone().index_add_(axis, idx, src)
def _segment_sum(src, seg, n):
    out = torch.zeros((n,) + src.shape[1:], dtype=src.dtype)
    return out.index_add_(0, seg, src)
def _pad(x, pw):
    flat = tuple(v for pair in reversed(pw) for v in pair)
    return torch.nn.functional.pad(x, flat)

kernel_name = 'concat_heads__ssm_conv_s4d__ssm_conv_s4d__prompt'

def setup_inputs():

    _w = (lambda a: (lambda t: t.to(torch.bfloat16) if t.is_floating_point() else t)(torch.as_tensor(a)))
    def _A_setup():
        Bsz, L, D, N = 1, 2048, 576, 128
        return {
            "u": _w((lambda *s: torch.randn(*s, dtype=torch.bfloat16) * 0.05)(Bsz, L, D)),
            "log_A_real": _w((lambda *s: torch.randn(*s, dtype=torch.bfloat16) * 6.4)(D, N) * 0.5),
            "A_imag": _w((lambda *s: torch.randn(*s, dtype=torch.bfloat16) * 0.05)(D, N) * 3.14),
            "C_real": _w((lambda *s: torch.randn(*s, dtype=torch.bfloat16) * 0.05)(D, N) / N ** 0.5),
            "C_imag": _w((lambda *s: torch.randn(*s, dtype=torch.bfloat16) * 0.05)(D, N) / N ** 0.5),
            "log_dt": _w((lambda *s: torch.randn(*s, dtype=torch.bfloat16) * 0.05)(D) * 0.5 - 4.0),
            "D_skip": _w((lambda *s: torch.randn(*s, dtype=torch.bfloat16) * 0.05)(D)),
        }
    _A_in = _A_setup()

    def _B_setup():
        Bsz, L, D, N = 1, 2048, 576, 128
        return {
            "u": _w((lambda *s: torch.randn(*s, dtype=torch.bfloat16) * 0.05)(Bsz, L, D)),
            "log_A_real": _w((lambda *s: torch.randn(*s, dtype=torch.bfloat16) * 6.4)(D, N) * 0.5),
            "A_imag": _w((lambda *s: torch.randn(*s, dtype=torch.bfloat16) * 0.05)(D, N) * 3.14),
            "C_real": _w((lambda *s: torch.randn(*s, dtype=torch.bfloat16) * 0.05)(D, N) / N ** 0.5),
            "C_imag": _w((lambda *s: torch.randn(*s, dtype=torch.bfloat16) * 0.05)(D, N) / N ** 0.5),
            "log_dt": _w((lambda *s: torch.randn(*s, dtype=torch.bfloat16) * 0.05)(D) * 0.5 - 4.0),
            "D_skip": _w((lambda *s: torch.randn(*s, dtype=torch.bfloat16) * 0.05)(D)),
        }
    _B_in = _B_setup()

    return {
        "A_u": _A_in["u"],
        "A_log_A_real": _A_in["log_A_real"],
        "A_A_imag": _A_in["A_imag"],
        "A_C_real": _A_in["C_real"],
        "A_C_imag": _A_in["C_imag"],
        "A_log_dt": _A_in["log_dt"],
        "A_D_skip": _A_in["D_skip"],
        "B_u": _B_in["u"],
        "B_log_A_real": _B_in["log_A_real"],
        "B_A_imag": _B_in["A_imag"],
        "B_C_real": _B_in["C_real"],
        "B_C_imag": _B_in["C_imag"],
        "B_log_dt": _B_in["log_dt"],
        "B_D_skip": _B_in["D_skip"],
    }

def reference(A_u, A_log_A_real, A_A_imag, A_C_real, A_C_imag, A_log_dt, A_D_skip, B_u, B_log_A_real, B_A_imag, B_C_real, B_C_imag, B_log_dt, B_D_skip):
    def _A_ref(u, log_A_real, A_imag, C_real, C_imag, log_dt, D_skip):
        L = u.shape[1]
        dt = torch.exp(log_dt)
        A = -torch.exp(log_A_real) + 1j * A_imag
        C = (C_real + 1j * C_imag) * dt[:, None]
        pos = (lambda x, dt: x.to(dt))(torch.arange(L), log_dt.dtype)
        P = torch.exp((A * dt[:, None])[:, :, None] * pos[None, None, :])
        K = 2.0 * torch.einsum("dn,dnl->dl", C, P).real
        L2 = 2 * L
        u_t = (lambda x, *d: x.permute(*d))(u, 0, 2, 1)
        Kf = (lambda x, *a, **k: torch.fft.rfft(x.float(), *a, **k))(K, L2)
        Uf = (lambda x, *a, **k: torch.fft.rfft(x.float(), *a, **k))(u_t, L2)
        y = (lambda x, *a, **k: torch.fft.irfft(x, *a, **k).to(torch.bfloat16))(Uf * Kf[None, :, :], L2)[:, :, :L]
        y = (lambda x, *d: x.permute(*d))(y, 0, 2, 1)
        return y + u * D_skip[None, None, :]

    def _B_ref(u, log_A_real, A_imag, C_real, C_imag, log_dt, D_skip):
        L = u.shape[1]
        dt = torch.exp(log_dt)
        A = -torch.exp(log_A_real) + 1j * A_imag
        C = (C_real + 1j * C_imag) * dt[:, None]
        pos = (lambda x, dt: x.to(dt))(torch.arange(L), log_dt.dtype)
        P = torch.exp((A * dt[:, None])[:, :, None] * pos[None, None, :])
        K = 2.0 * torch.einsum("dn,dnl->dl", C, P).real
        L2 = 2 * L
        u_t = (lambda x, *d: x.permute(*d))(u, 0, 2, 1)
        Kf = (lambda x, *a, **k: torch.fft.rfft(x.float(), *a, **k))(K, L2)
        Uf = (lambda x, *a, **k: torch.fft.rfft(x.float(), *a, **k))(u_t, L2)
        y = (lambda x, *a, **k: torch.fft.irfft(x, *a, **k).to(torch.bfloat16))(Uf * Kf[None, :, :], L2)[:, :, :L]
        y = (lambda x, *d: x.permute(*d))(y, 0, 2, 1)
        return y + u * D_skip[None, None, :]

    a = _A_ref(A_u, A_log_A_real, A_A_imag, A_C_real, A_C_imag, A_log_dt, A_D_skip)
    b = _B_ref(B_u, B_log_A_real, B_A_imag, B_C_real, B_C_imag, B_log_dt, B_D_skip)
    return torch.concatenate([a, b], axis=-1)
```

```cpp
#include <hip/hip_runtime.h>
#include <hip/hip_bf16.h>
#include <stdint.h>

#define LL 2048
#define DD 576
#define NN 128

static __device__ __forceinline__ float bf2f(uint16_t x){ return __uint_as_float(((uint32_t)x)<<16); }
static __device__ __forceinline__ uint16_t f2bf(float f){ uint32_t u=__float_as_uint(f); u += 0x7fff + ((u>>16)&1); return (uint16_t)(u>>16); }
static __device__ __forceinline__ float rbf(float f){ return bf2f(f2bf(f)); }

__global__ void kgen(const uint16_t* lar, const uint16_t* aim, const uint16_t* cr, const uint16_t* ci, const uint16_t* ldt, float* K){
  int d = blockIdx.x;
  __shared__ float sar[NN], sai[NN], scr[NN], sci[NN];
  float dt = rbf(expf(bf2f(ldt[d])));
  if (threadIdx.x < NN){
    int n = threadIdx.x;
    float ar = -rbf(expf(bf2f(lar[d*NN+n])));
    sar[n] = ar*dt; sai[n] = bf2f(aim[d*NN+n])*dt;
    scr[n] = bf2f(cr[d*NN+n])*dt; sci[n] = bf2f(ci[d*NN+n])*dt;
  }
  __syncthreads();
  for (int l = threadIdx.x; l < LL; l += blockDim.x){
    float p = rbf((float)l);
    float acc = 0.f;
    for (int n=0;n<NN;n++){
      float e = expf(sar[n]*p);
      float s,c; sincosf(sai[n]*p,&s,&c);
      acc += e*(scr[n]*c - sci[n]*s);
    }
    K[d*LL+l] = 2.f*acc;
  }
}

__global__ void conv(const uint16_t* u, const uint16_t* dsk, const float* K, uint16_t* out, int off){
  int d = blockIdx.x;
  __shared__ float su[LL], sk[LL];
  for (int l = threadIdx.x; l < LL; l += blockDim.x){ su[l]=bf2f(u[l*DD+d]); sk[l]=K[d*LL+l]; }
  __syncthreads();
  float D = bf2f(dsk[d]);
  for (int t = threadIdx.x; t < LL; t += blockDim.x){
    float acc=0.f;
    for (int l=0;l<=t;l++) acc += sk[l]*su[t-l];
    float y = rbf(acc);
    float s = rbf(su[t]*D);
    out[t*(2*DD)+off+d] = f2bf(y+s);
  }
}

extern "C" void kernel_launch(void* const* d_in, const int* in_sizes, int n_in,
                              void* d_out, int out_size, void* d_ws, size_t ws_size,
                              hipStream_t stream) {
  float* K = (float*)d_ws;
  for (int b=0;b<2;b++){
    const uint16_t* u=(const uint16_t*)d_in[7*b+0];
    const uint16_t* lar=(const uint16_t*)d_in[7*b+1];
    const uint16_t* aim=(const uint16_t*)d_in[7*b+2];
    const uint16_t* cr=(const uint16_t*)d_in[7*b+3];
    const uint16_t* ci=(const uint16_t*)d_in[7*b+4];
    const uint16_t* ldt=(const uint16_t*)d_in[7*b+5];
    const uint16_t* dsk=(const uint16_t*)d_in[7*b+6];
    kgen<<<DD,256,0,stream>>>(lar,aim,cr,ci,ldt,K+(size_t)b*DD*LL);
    conv<<<DD,256,0,stream>>>(u,dsk,K+(size_t)b*DD*LL,(uint16_t*)d_out,b*DD);
  }
}
```

```cpp
#include <hip/hip_runtime.h>
#include <hip/hip_bf16.h>
#include <stdint.h>

typedef __bf16   bf16x8 __attribute__((ext_vector_type(8)));
typedef __bf16   bf16x2 __attribute__((ext_vector_type(2)));
typedef _Float16 f16x8  __attribute__((ext_vector_type(8)));
typedef _Float16 f16x2  __attribute__((ext_vector_type(2)));
typedef float    f32x4  __attribute__((ext_vector_type(4)));
typedef float    f32x2  __attribute__((ext_vector_type(2)));
typedef uint32_t u32x4  __attribute__((ext_vector_type(4)));
typedef uint32_t u32x2  __attribute__((ext_vector_type(2)));
typedef short    s16x4  __attribute__((ext_vector_type(4)));

#define LDSP(T, off) ((__attribute__((address_space(3))) T*)(uintptr_t)(off))
#define CBAR() asm volatile("" ::: "memory")

constexpr int DH = 576, DO = 1152;
constexpr int U_STRIDE = 4576;
constexpr int U_OFF    = 0;
constexpr int ST_TILE  = 4608;
constexpr int ST_OFF   = U_OFF + 8 * U_STRIDE;
constexpr int KS_BYTES = 4608;
constexpr int KS_OFF   = ST_OFF + 8 * ST_TILE;
constexpr int LDS_BYTES = KS_OFF + 8 * KS_BYTES;
constexpr int T_RO = 0, T_RE = 1216, T_E3 = 2336, T_O3 = 2592, T_E4A = 2848, T_O4A = 3008, T_E4B = 3168, T_O4B = 3328;
constexpr int T_DUMMY = 3584;

__device__ __forceinline__ float bfl(uint32_t x) { return __uint_as_float(x << 16); }
__device__ __forceinline__ float bfh(uint32_t x) { return __uint_as_float(x & 0xffff0000u); }
__device__ __forceinline__ uint32_t pkbf(float lo, float hi) {
    f32x2 v = {lo, hi};
    return __builtin_bit_cast(uint32_t, __builtin_convertvector(v, bf16x2));
}
__device__ __forceinline__ uint32_t pkh(float lo, float hi) {
    f32x2 v = {lo, hi};
    return __builtin_bit_cast(uint32_t, __builtin_convertvector(v, f16x2));
}
__device__ __forceinline__ float rbf(float x) { return bfl(pkbf(x, x) & 0xffffu); }
__device__ __forceinline__ uint32_t cmul(uint32_t x, uint32_t w) {
    uint32_t t, r;
    asm("v_pk_mul_f16 %0, %1, %2 op_sel:[0,0] op_sel_hi:[1,0]" : "=v"(t) : "v"(x), "v"(w));
    asm("v_pk_fma_f16 %0, %1, %2, %3 op_sel:[1,1,0] op_sel_hi:[0,1,1] neg_lo:[0,1,0]" : "=v"(r) : "v"(x), "v"(w), "v"(t));
    return r;
}
struct c32 { float r, i; };
__device__ __forceinline__ c32 csq(c32 a) { c32 o; o.r = __builtin_fmaf(a.r, a.r, -(a.i * a.i)); o.i = (a.r + a.r) * a.i; return o; }
__device__ __forceinline__ c32 cexp_pos(float a2, float rev, float p) {
    float m = __builtin_amdgcn_exp2f(a2 * p);
    float ph = __builtin_amdgcn_fractf(rev * p);
    c32 o; o.r = m * __builtin_amdgcn_cosf(ph); o.i = m * __builtin_amdgcn_sinf(ph); return o;
}
__device__ __forceinline__ f32x4 mma_bf(u32x4 a, u32x4 b, f32x4 c) {
    return __builtin_amdgcn_mfma_f32_16x16x32_bf16(__builtin_bit_cast(bf16x8, a), __builtin_bit_cast(bf16x8, b), c, 0, 0, 0);
}
__device__ __forceinline__ f32x4 mma_h(u32x4 a, u32x4 b, f32x4 c) {
    return __builtin_amdgcn_mfma_f32_16x16x32_f16(__builtin_bit_cast(f16x8, a), __builtin_bit_cast(f16x8, b), c, 0, 0, 0);
}
__device__ __forceinline__ u32x4 band(int d0, int lo, int n, uint32_t val) {
    uint64_t full = (uint64_t)((1u << n) - 1u) << (lo + 16);
    uint32_t M = (uint32_t)(full >> (d0 + 16));
    u32x4 o;
#pragma unroll
    for (int p = 0; p < 4; p++) {
        uint32_t b = (M >> (2 * p)) & 3u;
        uint32_t t = (b | (b << 15)) & 0x00010001u;
        o[p] = t * val;
    }
    return o;
}

template <int S>
__device__ __forceinline__ void conv_step(uint32_t ub, uint32_t ub3, f32x4 (&acc)[8], const u32x4 (&T)[17], const u32x4& T32,
                                          const u32x4 (&A3)[6], const u32x4 (&A4)[4], const u32x4& B5, const u32x4& B3,
                                          const u32x4& B9, const u32x4& B7, uint32_t stw) {
    const f32x4 z = {0.f, 0.f, 0.f, 0.f};
    f32x4 d7 = z;
#pragma unroll
    for (int r = 7; r >= 0; r--) {
        const int m = 8 * S - r;
        u32x4 U = *LDSP(u32x4, ub + 448 + 64 * m);
        acc[S] = mma_bf(U, T[r], acc[S]);
        if (S + 1 <= 7) acc[(S + 1) & 7] = mma_bf(U, T[8 + r], acc[(S + 1) & 7]);
        if (r == 0) {
            if (S + 2 <= 7) acc[(S + 2) & 7] = mma_bf(U, T[16], acc[(S + 2) & 7]);
            if (S + 4 <= 7) { acc[(S + 4) & 7] = mma_bf(U, T32, acc[(S + 4) & 7]); d7 = mma_bf(U, B7, z); }
        }
    }
    {
        u32x2 o; o[0] = pkbf(acc[S][0], acc[S][1]); o[1] = pkbf(acc[S][2], acc[S][3]);
        *LDSP(u32x2, stw + S * ST_TILE) = o;
    }
    if (S <= 3) {
        u32x4 Ue = *LDSP(u32x4, ub + 432 + 512 * S);
        f32x4 d9 = mma_bf(Ue, B9, z);
        u32x4 W; W[0] = pkbf(d9[0], d9[1]); W[1] = pkbf(d9[2], d9[3]); W[2] = pkbf(d7[0], d7[1]); W[3] = pkbf(d7[2], d7[3]);
#pragma unroll
        for (int nu = 4; nu <= 7; nu++)
            if (S + nu <= 7) acc[(S + nu) & 7] = mma_bf(A4[nu - 4], W, acc[(S + nu) & 7]);
    }
    if (S <= 5) {
#pragma unroll
        for (int h = 0; h < 2; h++) {
            u32x4 U3 = *LDSP(u32x4, ub3 + 432 + 256 * (2 * S + h));
            f32x4 d5 = mma_bf(U3, B5, z), d3 = mma_bf(U3, B3, z);
            u32x4 W; W[0] = pkbf(d5[0], d5[1]); W[1] = pkbf(d5[2], d5[3]); W[2] = pkbf(d3[0], d3[1]); W[3] = pkbf(d3[2], d3[3]);
#pragma unroll
            for (int q = 2; q <= 4; q++)
                if (S + q <= 7) acc[(S + q) & 7] = mma_bf(A3[2 * q - h - 3], W, acc[(S + q) & 7]);
        }
    }
}

__global__ __launch_bounds__(512, 2) void s4d_conv_kernel(
    const uint16_t* __restrict__ uA, const uint16_t* __restrict__ larA, const uint16_t* __restrict__ aimA,
    const uint16_t* __restrict__ crA, const uint16_t* __restrict__ ciA, const uint16_t* __restrict__ ldtA,
    const uint16_t* __restrict__ dsA,
    const uint16_t* __restrict__ uB, const uint16_t* __restrict__ larB, const uint16_t* __restrict__ aimB,
    const uint16_t* __restrict__ crB, const uint16_t* __restrict__ ciB, const uint16_t* __restrict__ ldtB,
    const uint16_t* __restrict__ dsB, uint16_t* __restrict__ out) {
    __shared__ __attribute__((aligned(16))) char smem[LDS_BYTES];
    const uint32_t sb = (uint32_t)(uintptr_t)(__attribute__((address_space(3))) char*)smem;
    const int tid = threadIdx.x, lane = tid & 63;
    const int wave = __builtin_amdgcn_readfirstlane(tid >> 6);
    const int b = blockIdx.x;
    const int grp = (b & 7) * 18 + (b >> 3);
    const bool hb = grp >= 72;
    const int c0 = 8 * (hb ? grp - 72 : grp);
    const int ch = c0 + wave;
    const uint16_t* u = hb ? uB : uA;
    const uint16_t* lar = hb ? larB : larA;
    const uint16_t* aim = hb ? aimB : aimA;
    const uint16_t* cr = hb ? crB : crA;
    const uint16_t* ci = hb ? ciB : ciA;
    const uint16_t* ldt = hb ? ldtB : ldtA;
    const uint16_t* dsk = hb ? dsB : dsA;

    const uint32_t p_lar = ((const uint32_t*)lar)[ch * 64 + lane];
    const uint32_t p_aim = ((const uint32_t*)aim)[ch * 64 + lane];
    const uint32_t p_cr  = ((const uint32_t*)cr)[ch * 64 + lane];
    const uint32_t p_ci  = ((const uint32_t*)ci)[ch * 64 + lane];
    const uint32_t p_ldt = ldt[ch];
    const uint32_t p_dsk = dsk[ch];
    const uint16_t* up = u + (size_t)(2 * tid) * DH + c0;
    const u32x4 r0 = *(const u32x4*)(up);
    const u32x4 r1 = *(const u32x4*)(up + DH);
    const u32x4 r2 = *(const u32x4*)(up + 1024 * DH);
    const u32x4 r3 = *(const u32x4*)(up + 1025 * DH);

    const int phi = lane & 15, g = lane >> 4;
    const int d0 = 8 * g - phi;
    const u32x4 B5 = band(d0, 6, 5, 0x3F80u), B3 = band(d0, 11, 3, 0x3F80u);
    const u32x4 B9 = band(d0, 4, 9, 0x3F80u), B7 = band(d0, 5, 7, 0x3F80u);
    const u32x4 M32 = band(d0, 12, 2, 0xFFFFu);
    const uint32_t uch = sb + U_OFF + wave * U_STRIDE;
    *LDSP(uint32_t, uch + 4 * lane) = 0u;
    if (lane < 56) *LDSP(uint32_t, uch + 256 + 4 * lane) = 0u;

    const uint32_t ks = sb + KS_OFF + wave * KS_BYTES;
    const float dt = rbf(__builtin_amdgcn_exp2f(bfl(p_ldt) * 1.44269504088896341f));
    uint32_t mx = max(max(p_cr & 0x7fffu, (p_cr >> 16) & 0x7fffu), max(p_ci & 0x7fffu, (p_ci >> 16) & 0x7fffu));
    mx = max(mx, (uint32_t)__builtin_amdgcn_update_dpp(0, (int)mx, 0x111, 0xf, 0xf, true));
    mx = max(mx, (uint32_t)__builtin_amdgcn_update_dpp(0, (int)mx, 0x112, 0xf, 0xf, true));
    mx = max(mx, (uint32_t)__builtin_amdgcn_update_dpp(0, (int)mx, 0x114, 0xf, 0xf, true));
    mx = max(mx, (uint32_t)__builtin_amdgcn_update_dpp(0, (int)mx, 0x118, 0xf, 0xf, true));
    mx = max(mx, (uint32_t)__builtin_amdgcn_update_dpp(0, (int)mx, 0x142, 0xa, 0xf, true));
    mx = max(mx, (uint32_t)__builtin_amdgcn_update_dpp(0, (int)mx, 0x143, 0xc, 0xf, true));
    uint32_t ex = (uint32_t)__builtin_amdgcn_readlane((int)mx, 63) >> 7;
    ex = min(max(ex, 1u), 252u);
    const float cs = __uint_as_float((253u - ex) << 23);
    const float kf = 2.0f * dt * __uint_as_float((ex + 1u) << 23);

    f32x4 kA = {0.f, 0.f, 0.f, 0.f}, kB = kA, kC = kA;
    const uint32_t ksw = ks + 4 * lane;
    const uint32_t ksr = ks + 288 * phi + 16 * g;
#define ROW(r, v) (*LDSP(uint32_t, ksw + 288 * (r)) = (v))
#pragma unroll
    for (int p = 0; p < 2; p++) {
        const float vl = p ? bfh(p_lar) : bfl(p_lar), vi = p ? bfh(p_aim) : bfl(p_aim);
        const float vr = p ? bfh(p_cr) : bfl(p_cr), vc = p ? bfh(p_ci) : bfl(p_ci);
        const float ar = -rbf(__builtin_amdgcn_exp2f(vl * 1.44269504088896341f)) * dt;
        const float a2 = ar * 1.44269504088896341f;
        const float rev = (vi * dt) * 0.15915494309189535f;
        const c32 w1 = cexp_pos(a2, rev, 1.0f), w2 = csq(w1), w4 = csq(w2), w8 = csq(w4), w16 = csq(w8);
        const c32 w32 = cexp_pos(a2, rev, 32.0f), w64 = csq(w32), w128 = csq(w64), w256 = csq(w128), w512 = csq(w256), w1024 = csq(w512);
        uint32_t y[16];
        y[0] = 0x00003C00u; y[1] = pkh(w8.r, -w8.i); y[2] = pkh(w16.r, -w16.i); y[4] = pkh(w32.r, -w32.i); y[8] = pkh(w64.r, -w64.i);
        y[3] = cmul(y[2], y[1]); y[5] = cmul(y[4], y[1]); y[6] = cmul(y[4], y[2]); y[7] = cmul(y[4], y[3]);
#pragma unroll
        for (int j = 1; j < 8; j++) y[8 + j] = cmul(y[8], y[j]);
#pragma unroll
        for (int j = 0; j < 16; j++) ROW(j, y[j]);
        CBAR();
        u32x4 Y[4];
#pragma unroll
        for (int s = 0; s < 4; s++) Y[s] = *LDSP(u32x4, ksr + 64 * s);
        CBAR();
        const uint32_t h1 = pkh(w1.r, w1.i), h2 = pkh(w2.r, w2.i), h4 = pkh(w4.r, w4.i);
        const uint32_t h128 = pkh(w128.r, w128.i), h256 = pkh(w256.r, w256.i), h512 = pkh(w512.r, w512.i), h1024 = pkh(w1024.r, w1024.i);
        uint32_t x[8];
        x[0] = pkh(vr * cs, vc * cs);
        x[1] = cmul(x[0], h1); x[2] = cmul(x[0], h2); x[3] = cmul(x[1], h2);
#pragma unroll
        for (int q = 0; q < 4; q++) x[4 + q] = cmul(x[q], h4);
#pragma unroll
        for (int q = 0; q < 8; q++) { ROW(q, x[q]); ROW(8 + q, cmul(x[q], h128)); }
        CBAR();
#pragma unroll
        for (int s = 0; s < 4; s++) kA = mma_h(*LDSP(u32x4, ksr + 64 * s), Y[s], kA);
        CBAR();
#pragma unroll
        for (int q = 0; q < 4; q++) { uint32_t t = cmul(x[2 * q], h256); ROW(q, t); ROW(4 + q, cmul(t, h128)); }
#pragma unroll
        for (int q = 0; q < 2; q++) {
            uint32_t t0 = cmul(x[4 * q], h512), t1 = cmul(t0, h128);
            ROW(8 + q, t0); ROW(10 + q, t1); ROW(12 + q, cmul(t0, h256)); ROW(14 + q, cmul(t1, h256));
        }
        CBAR();
#pragma unroll
        for (int s = 0; s < 4; s++) kB = mma_h(*LDSP(u32x4, ksr + 64 * s), Y[s], kB);
        CBAR();
        {
            uint32_t t0 = cmul(x[0], h1024), t1 = cmul(t0, h128), t2 = cmul(t0, h256), t3 = cmul(t1, h256);
            uint32_t t7 = cmul(t3, h512);
            ROW(0, t0); ROW(1, t1); ROW(2, t2); ROW(3, t3);
            ROW(4, cmul(t0, h512)); ROW(5, cmul(t1, h512)); ROW(6, cmul(t2, h512)); ROW(7, t7);
            ROW(8, cmul(t7, h128));
        }
        CBAR();
#pragma unroll
        for (int s = 0; s < 4; s++) kC = mma_h(*LDSP(u32x4, ksr + 64 * s), Y[s], kC);
        CBAR();
    }
#undef ROW
    {
        const u32x4 z4 = {0u, 0u, 0u, 0u};
#pragma unroll
        for (int q = 0; q < 4; q++) *LDSP(u32x4, ks + 1024 * q + 16 * lane) = z4;
        CBAR();
        kA *= kf; kB *= kf; kC *= kf;
        if (lane == 0) kA[0] += bfl(p_dsk);
        const uint32_t b10 = pkbf(kB[1], kB[0]), b32_ = pkbf(kB[3], kB[2]);
        const uint32_t v0v0 = __builtin_amdgcn_perm(b10, b10, 0x03020302u), v2v2 = __builtin_amdgcn_perm(b32_, b32_, 0x03020302u);
        const uint32_t v3v2 = b32_, v1v0 = b10;
        const uint32_t v2v1 = __builtin_amdgcn_perm(b10, b32_, 0x05040302u);
        if (g < 2) {
            const int P0 = 256 + 128 * g + 8 * phi;
            const uint32_t ae = ks + T_RE + 2 * (538 - P0);
            *LDSP(uint32_t, ae) = v3v2; *LDSP(uint32_t, ae + 4) = v2v2; *LDSP(uint32_t, ae + 8) = v1v0; *LDSP(uint32_t, ae + 12) = v0v0;
            const uint32_t ao = ks + T_RO + 2 * (536 - P0);
            *LDSP(uint16_t, ao + 2) = (uint16_t)v3v2;
            *LDSP(uint32_t, ao + 4) = v2v2;
            *LDSP(uint32_t, ao + 8) = v2v1;
            *LDSP(uint32_t, ao + 12) = v0v0;
            *LDSP(uint16_t, ao + 16) = (uint16_t)v0v0;
        } else {
            const int i0 = 2 * (g - 2);
            const uint32_t ae = ks + T_E3 + 2 * (96 - 16 * i0 - phi), ao = ks + T_O3 + 2 * (95 - 16 * i0 - phi);
            *LDSP(uint16_t, (lane == 32) ? ks + T_DUMMY : ae) = (uint16_t)(b10 >> 16);
            *LDSP(uint16_t, ao) = (uint16_t)b10;
            *LDSP(uint16_t, ae - 32) = (uint16_t)(b32_ >> 16);
            *LDSP(uint16_t, ao - 32) = (uint16_t)b32_;
        }
        CBAR();
        {
            const int l0 = 128 * (g >> 1) + 8 * phi + 4 * (g & 1);
            const uint32_t a10 = pkbf(kA[1], kA[0]), a32 = pkbf(kA[3], kA[2]);
            *LDSP(u32x2, ks + T_RO + 2 * (540 - l0)) = (u32x2){a32, a10};
            const uint32_t ae = ks + T_RE + 2 * (540 - l0);
            *LDSP(uint16_t, ae + 2) = (uint16_t)a32;
            *LDSP(uint32_t, ae + 4) = __builtin_amdgcn_perm(a10, a32, 0x05040302u);
            *LDSP(uint16_t, ae + 8) = (uint16_t)(a10 >> 16);
        }
        const uint32_t c10 = pkbf(kC[1], kC[0]), c32_ = pkbf(kC[3], kC[2]);
        if (g < 2) {
            const int m0 = 64 + 32 * g + (phi >> 1);
            const bool od = phi & 1;
            const uint32_t aa = ks + (od ? T_O4A + 2 * (128 - m0) : T_E4A + 2 * (129 - m0));
            const uint32_t ab = ks + (od ? T_O4B + 2 * (127 - m0) : T_E4B + 2 * (128 - m0));
            *LDSP(uint16_t, (lane == 0) ? ks + T_DUMMY : aa) = (uint16_t)(c10 >> 16);
            *LDSP(uint16_t, (lane == 0) ? ks + T_DUMMY : ab) = (uint16_t)(c10 >> 16);
            *LDSP(uint16_t, aa - 16) = (uint16_t)c10;          *LDSP(uint16_t, ab - 16) = (uint16_t)c10;
            *LDSP(uint16_t, aa - 32) = (uint16_t)(c32_ >> 16); *LDSP(uint16_t, ab - 32) = (uint16_t)(c32_ >> 16);
            *LDSP(uint16_t, aa - 48) = (uint16_t)c32_;         *LDSP(uint16_t, ab - 48) = (uint16_t)c32_;
        }
        CBAR();
        const uint32_t k512 = (uint32_t)__builtin_amdgcn_readlane((int)b10, 32) >> 16;
        const uint32_t k1024 = (uint32_t)__builtin_amdgcn_readlane((int)c10, 0) >> 16;
        const uint32_t k2048 = (uint32_t)__builtin_amdgcn_readlane((int)c10, 32) >> 16;
        if (lane == 0) {
            const uint32_t kk = k512 | (k512 << 16);
            *LDSP(uint32_t, ks + T_RE + 2 * 30) = kk;
            *LDSP(uint32_t, ks + T_RE + 2 * 32) = kk;
            *LDSP(uint16_t, ks + T_RO + 2 * 29) = (uint16_t)k512;
            *LDSP(uint32_t, ks + T_RO + 2 * 30) = kk;
            *LDSP(uint16_t, ks + T_RO + 2 * 32) = (uint16_t)k512;
            *LDSP(uint16_t, ks + T_E3 + 2 * 32) = (uint16_t)k1024;
            *LDSP(uint16_t, ks + T_E4A + 2 * 1) = (uint16_t)k2048;
            *LDSP(uint16_t, ks + T_E4B + 2 * 0) = (uint16_t)k2048;
        }
        CBAR();
        (void)0;
        u32x4 T[17], A3[6], A4[4], T32;
        {
            const uint32_t at = ks + ((phi & 1) ? T_RO + 2 * (527 - phi + 8 * g) : T_RE + 2 * (528 - phi + 8 * g)) - 1024;
#pragma unroll
            for (int n = 0; n <= 16; n++)
#pragma unroll
                for (int q = 0; q < 4; q++) T[n][q] = *LDSP(uint32_t, at + 64 * (16 - n) + 4 * q);
            const uint32_t a3 = ks + 4 * (79 - 64 - phi + 2 * g);
#pragma unroll
            for (int nu = 3; nu <= 8; nu++) {
                const int o = 32 * (8 - nu);
                A3[nu - 3][0] = *LDSP(uint32_t, a3 + T_E3 + o); A3[nu - 3][1] = *LDSP(uint32_t, a3 + T_E3 + o + 4);
                A3[nu - 3][2] = *LDSP(uint32_t, a3 + T_O3 + o); A3[nu - 3][3] = *LDSP(uint32_t, a3 + T_O3 + o + 4);
            }
            const uint32_t a4 = ks + 4 * (64 - 56 - ((phi + 1) >> 1) + 2 * g) + ((phi & 1) ? T_E4B - T_E4A : 0);
#pragma unroll
            for (int nu = 4; nu <= 7; nu++) {
                const int o = 32 * (7 - nu);
                A4[nu - 4][0] = *LDSP(uint32_t, a4 + T_E4A + o); A4[nu - 4][1] = *LDSP(uint32_t, a4 + T_E4A + o + 4);
                A4[nu - 4][2] = *LDSP(uint32_t, a4 + T_O4A + o); A4[nu - 4][3] = *LDSP(uint32_t, a4 + T_O4A + o + 4);
            }
            const uint32_t kk = k1024 | (k1024 << 16);
#pragma unroll
            for (int q = 0; q < 4; q++) T32[q] = M32[q] & kk;
        }
        {
            const uint32_t uw = sb + U_OFF + 480 + 4 * tid;
#pragma unroll
            for (int c = 0; c < 8; c++) {
                const uint32_t sel = (c & 1) ? 0x07060302u : 0x05040100u;
                *LDSP(uint32_t, uw + c * U_STRIDE) = __builtin_amdgcn_perm(r1[c >> 1], r0[c >> 1], sel);
                *LDSP(uint32_t, uw + c * U_STRIDE + 2048) = __builtin_amdgcn_perm(r3[c >> 1], r2[c >> 1], sel);
            }
        }
        __syncthreads();
        f32x4 acc[8];
#pragma unroll
        for (int q = 0; q < 8; q++) acc[q] = (f32x4){0.f, 0.f, 0.f, 0.f};
        const uint32_t ub = uch + 32 * phi + 16 * g, ub3 = uch + 16 * (phi + g);
        const uint32_t stw = sb + ST_OFF + wave * 576 + 8 * lane;
        const int wq = wave & 3;
        const uint32_t str = sb + ST_OFF + ((lane & 15) >> 2) * 576 + 2 * (64 * wq + 16 * g + 4 * (lane & 3));
        uint16_t* op = out + (size_t)(64 * wq + 16 * (lane & 3) + 4 * g + ((lane & 15) >> 2)) * DO + 8 * grp;
#define STEP(S)                                                                                             \
        conv_step<S>(ub, ub3, acc, T, T32, A3, A4, B5, B3, B9, B7, stw);                                    \
        __syncthreads();                                                                                    \
        if ((wave >> 2) == ((S) & 1)) {                                                                     \
            s16x4 lo = __builtin_amdgcn_ds_read_tr16_b64_v4i16(LDSP(s16x4, str + (S) * ST_TILE));           \
            s16x4 hi = __builtin_amdgcn_ds_read_tr16_b64_v4i16(LDSP(s16x4, str + (S) * ST_TILE + 4 * 576)); \
            u32x2 l2 = __builtin_bit_cast(u32x2, lo), h2 = __builtin_bit_cast(u32x2, hi);                   \
            *(u32x4*)(op + (size_t)(256 * (S)) * DO) = (u32x4){l2[0], l2[1], h2[0], h2[1]};                 \
        }
        STEP(0) STEP(1) STEP(2) STEP(3) STEP(4) STEP(5) STEP(6) STEP(7)
#undef STEP
    }
}

extern "C" void kernel_launch(void* const* d_in, const int* in_sizes, int n_in, void* d_out, int out_size, void* d_ws,
                              size_t ws_size, hipStream_t stream) {
    const uint16_t* const* in = (const uint16_t* const*)d_in;
    s4d_conv_kernel<<<144, 512, 0, stream>>>(in[0], in[1], in[2], in[3], in[4], in[5], in[6], in[7], in[8], in[9], in[10],
                                             in[11], in[12], in[13], (uint16_t*)d_out);
}
```

```cpp
#include <hip/hip_runtime.h>
#include <hip/hip_bf16.h>
#include <stdint.h>

typedef __bf16   bf16x8 __attribute__((ext_vector_type(8)));
typedef __bf16   bf16x2 __attribute__((ext_vector_type(2)));
typedef _Float16 f16x8  __attribute__((ext_vector_type(8)));
typedef _Float16 f16x2  __attribute__((ext_vector_type(2)));
typedef float    f32x4  __attribute__((ext_vector_type(4)));
typedef float    f32x2  __attribute__((ext_vector_type(2)));
typedef uint32_t u32x4  __attribute__((ext_vector_type(4)));
typedef uint32_t u32x2  __attribute__((ext_vector_type(2)));
typedef short    s16x4  __attribute__((ext_vector_type(4)));

#define LDSP(T, off) ((__attribute__((address_space(3))) T*)(uintptr_t)(off))
#define CBAR() asm volatile("" ::: "memory")

constexpr int DH = 576, DO = 1152;
constexpr int U_STRIDE = 4576;
constexpr int KS_ROW   = 544;
constexpr int KS_BYTES = 16 * KS_ROW + 64;
constexpr int KS_OFF   = 0;
constexpr int U_OFF    = KS_OFF + 8 * KS_BYTES;
constexpr int TB_BYTES = 4096;
constexpr int TB_OFF   = U_OFF + 8 * U_STRIDE;
constexpr int PG_OFF   = TB_OFF + 8 * TB_BYTES;
constexpr int LDS_BYTES = PG_OFF + 64;
constexpr int T_RO = 0, T_RE = 1216, T_E3 = 2336, T_O3 = 2592, T_E4A = 2848, T_O4A = 3008, T_E4B = 3168, T_O4B = 3328;
constexpr int T_DUMMY = 3584;

__device__ __forceinline__ float bfl(uint32_t x) { return __uint_as_float(x << 16); }
__device__ __forceinline__ float bfh(uint32_t x) { return __uint_as_float(x & 0xffff0000u); }
__device__ __forceinline__ uint32_t pkbf(float lo, float hi) {
    f32x2 v = {lo, hi};
    return __builtin_bit_cast(uint32_t, __builtin_convertvector(v, bf16x2));
}
__device__ __forceinline__ uint32_t pkh(float lo, float hi) {
    f32x2 v = {lo, hi};
    return __builtin_bit_cast(uint32_t, __builtin_convertvector(v, f16x2));
}
__device__ __forceinline__ float rbf(float x) { return bfl(pkbf(x, x) & 0xffffu); }
__device__ __forceinline__ uint32_t cmul(uint32_t xx, uint32_t ww) {
    const f16x2 x = __builtin_bit_cast(f16x2, xx), w = __builtin_bit_cast(f16x2, ww);
    const f16x2 wr = __builtin_shufflevector(w, w, 0, 0), wi = __builtin_shufflevector(w, w, 1, 1);
    const f16x2 xs = {-x.y, x.x};
    return __builtin_bit_cast(uint32_t, __builtin_elementwise_fma(xs, wi, x * wr));
}
struct c32 { f32x2 r, i; };
__device__ __forceinline__ c32 csq(c32 a) {
    c32 o; const f32x2 p = a.r * a.i;
    o.r = __builtin_elementwise_fma(a.r, a.r, -(a.i * a.i)); o.i = p + p; return o;
}
__device__ __forceinline__ c32 cexp_pos(f32x2 a2, f32x2 rev) {
    const f32x2 m = {__builtin_amdgcn_exp2f(a2.x), __builtin_amdgcn_exp2f(a2.y)};
    const float p0 = __builtin_amdgcn_fractf(rev.x), p1 = __builtin_amdgcn_fractf(rev.y);
    const f32x2 c = {__builtin_amdgcn_cosf(p0), __builtin_amdgcn_cosf(p1)}, s = {__builtin_amdgcn_sinf(p0), __builtin_amdgcn_sinf(p1)};
    c32 o; o.r = m * c; o.i = m * s; return o;
}
__device__ __forceinline__ f32x4 mma_bf(u32x4 a, u32x4 b, f32x4 c) {
    return __builtin_amdgcn_mfma_f32_16x16x32_bf16(__builtin_bit_cast(bf16x8, a), __builtin_bit_cast(bf16x8, b), c, 0, 0, 0);
}
__device__ __forceinline__ f32x4 mma_h(u32x4 a, u32x4 b, f32x4 c) {
    return __builtin_amdgcn_mfma_f32_16x16x32_f16(__builtin_bit_cast(f16x8, a), __builtin_bit_cast(f16x8, b), c, 0, 0, 0);
}
struct BandTab { uint32_t v[7 * 64 * 4]; };
constexpr BandTab make_bands() {
    BandTab t{};
    constexpr int lo[7] = {6, 11, 4, 5, 12, 14, -15}, n[7] = {5, 3, 9, 7, 2, 4, 1};
    for (int q = 0; q < 7; q++)
        for (int l = 0; l < 64; l++)
            for (int j = 0; j < 8; j++) {
                const int d = 8 * (l >> 4) + j - (l & 15);
                if (d >= lo[q] && d < lo[q] + n[q]) t.v[(q * 64 + l) * 4 + (j >> 1)] |= (q < 4 ? 0x3F80u : 0xFFFFu) << (16 * (j & 1));
            }
    return t;
}
static __device__ const __attribute__((aligned(16))) BandTab band_tab = make_bands();

#define SB() __builtin_amdgcn_sched_barrier(0)
#define WGBAR() asm volatile("s_waitcnt lgkmcnt(0)\n\ts_barrier" ::: "memory")
__device__ __forceinline__ void publish(uint32_t addr, uint32_t one) {
    asm volatile("s_mov_b64 exec, 1\n\tds_add_u32 %0, %1\n\ts_mov_b64 exec, -1" :: "v"(addr), "v"(one) : "memory");
}
template <int NEED>
__device__ __forceinline__ void await(uint32_t addr) {
    uint32_t v;
    asm volatile("1:\n\tds_read_b32 %0, %1\n\ts_waitcnt lgkmcnt(0)\n\tv_cmp_gt_u32 vcc, %2, %0\n\ts_cbranch_vccz 2f\n\ts_sleep 1\n\ts_branch 1b\n2:"
                 : "=&v"(v) : "v"(addr), "n"(NEED) : "vcc", "memory");
}
__device__ __forceinline__ void await_dyn(uint32_t addr, uint32_t need) {
    uint32_t v;
    asm volatile("1:\n\tds_read_b32 %0, %1\n\ts_waitcnt lgkmcnt(0)\n\tv_cmp_gt_u32 vcc, %2, %0\n\ts_cbranch_vccz 2f\n\ts_sleep 1\n\ts_branch 1b\n2:"
                 : "=&v"(v) : "v"(addr), "s"(need) : "vcc", "memory");
}
template <int H, int P>
__device__ __forceinline__ void prio_if(int half) {
    asm volatile("s_cmp_lg_u32 %0, %1\n\ts_cbranch_scc1 1f\n\ts_setprio %2\n1:" :: "s"(half), "n"(H), "n"(P) : "scc");
}

constexpr int ngrp(int S) { return S <= 3 ? 11 : (S <= 5 ? 10 : 8); }
constexpr int gbase(int S) { int b = 0; for (int s = 0; s < S; s++) b += ngrp(s); return b; }
constexpr int gkind(int S, int k) {
    if (k == 0) return 0;
    if (S <= 3) { if (k == 1) return 8; if (k == 2) return 9; if (k == 3) return 10; return k - 3; }
    if (S <= 5) { if (k == 1) return 9; if (k == 2) return 10; return k - 2; }
    return k;
}
constexpr int gstep(int I) { int s = 0; while (s < 8 && I >= gbase(s) + ngrp(s)) s++; return s; }

struct Frags {
    u32x4 T[17], T32, A3[6], A4[4], B5, B3, B9, B7;
};
struct Pipe {
    u32x4 R[4];
    f32x4 acc[8], d9, d7, d5[2], d3[2];
    u32x4 W4, W3[2];
    uint32_t ub, ub3, stw, pa, one, pw;
};
template <int I>
__device__ __forceinline__ void prefetch(Pipe& P) {
    constexpr int S = gstep(I);
    if constexpr (S < 8) {
        constexpr int kd = gkind(S, I - gbase(S));
        if constexpr (kd < 8) P.R[I & 3] = *LDSP(u32x4, P.ub + 448 + 64 * (8 * S - kd));
        else if constexpr (kd == 8) P.R[I & 3] = *LDSP(u32x4, P.ub + 432 + 512 * S);
        else P.R[I & 3] = *LDSP(u32x4, P.ub3 + 432 + 256 * (2 * S + kd - 9));
    }
}
__device__ __forceinline__ u32x4 pk4(f32x4 a, f32x4 b) {
    u32x4 W; W[0] = pkbf(a[0], a[1]); W[1] = pkbf(a[2], a[3]); W[2] = pkbf(b[0], b[1]); W[3] = pkbf(b[2], b[3]); return W;
}
template <int S, int K>
__device__ __forceinline__ void group(Pipe& P, const Frags& F) {
    constexpr int I = gbase(S) + K, kd = gkind(S, K);
    const f32x4 z = {0.f, 0.f, 0.f, 0.f};
    if constexpr (I + 3 == gbase(4)) await<1>(P.pw);
    prefetch<I + 3>(P);
    const u32x4 U = P.R[I & 3];
    if constexpr (kd < 8) {
        P.acc[S] = mma_bf(U, F.T[kd], (S == 0 && K == 0) ? z : P.acc[S]);
        if constexpr (S + 1 <= 7) P.acc[S + 1] = mma_bf(U, F.T[8 + kd], (S == 0 && K == 0) ? z : P.acc[S + 1]);
        if constexpr (kd == 0) {
            if constexpr (S + 2 <= 7) P.acc[S + 2] = mma_bf(U, F.T[16], S == 0 ? z : P.acc[S + 2]);
            if constexpr (S + 4 <= 7) { P.acc[S + 4] = mma_bf(U, F.T32, S == 0 ? z : P.acc[S + 4]); P.d7 = mma_bf(U, F.B7, z); }
        }
    } else if constexpr (kd == 8) {
        P.d9 = mma_bf(U, F.B9, z);
    } else {
        P.d5[kd - 9] = mma_bf(U, F.B5, z); P.d3[kd - 9] = mma_bf(U, F.B3, z);
    }
    constexpr int k4 = 4, k3a = (S <= 3) ? 5 : 4, k3b = k3a + 1;
    if constexpr (S <= 3 && K == k4) P.W4 = pk4(P.d9, P.d7);
    if constexpr (S <= 5 && K == k3a) P.W3[0] = pk4(P.d5[0], P.d3[0]);
    if constexpr (S <= 5 && K == k3b) P.W3[1] = pk4(P.d5[1], P.d3[1]);
    SB();
}
template <int S, int K>
__device__ __forceinline__ void groups(Pipe& P, const Frags& F) {
    if constexpr (K < ngrp(S)) { group<S, K>(P, F); groups<S, K + 1>(P, F); }
}
template <int S>
__device__ __forceinline__ void conv_step(Pipe& P, const Frags& F) {
    const f32x4 z = {0.f, 0.f, 0.f, 0.f};
    groups<S, 0>(P, F);
    if constexpr (S <= 5) {
        P.acc[S + 2] = mma_bf(F.A3[1], P.W3[0], P.acc[S + 2]);
        P.acc[S + 2] = mma_bf(F.A3[0], P.W3[1], P.acc[S + 2]);
    }
    {
        u32x2 o; o[0] = pkbf(P.acc[S][0], P.acc[S][1]); o[1] = pkbf(P.acc[S][2], P.acc[S][3]);
        *LDSP(u32x2, P.stw + S * 512) = o;
        publish(P.pa, P.one);
    }
    SB();
    if constexpr (S <= 5) {
#pragma unroll
        for (int q = 3; q <= 4; q++)
            if (S + q <= 7) {
                P.acc[(S + q) & 7] = mma_bf(F.A3[2 * q - 3], P.W3[0], (S == 0 && q == 3) ? z : P.acc[(S + q) & 7]);
                P.acc[(S + q) & 7] = mma_bf(F.A3[2 * q - 4], P.W3[1], P.acc[(S + q) & 7]);
            }
    }
    if constexpr (S <= 3) {
#pragma unroll
        for (int nu = 4; nu <= 7; nu++)
            if (S + nu <= 7) P.acc[(S + nu) & 7] = mma_bf(F.A4[nu - 4], P.W4, (S == 0 && nu >= 5) ? z : P.acc[(S + nu) & 7]);
    }
    SB();
}

__global__ __launch_bounds__(512, 2) void s4d_conv_kernel(
    const uint16_t* __restrict__ uA, const uint16_t* __restrict__ larA, const uint16_t* __restrict__ aimA,
    const uint16_t* __restrict__ crA, const uint16_t* __restrict__ ciA, const uint16_t* __restrict__ ldtA,
    const uint16_t* __restrict__ dsA,
    const uint16_t* __restrict__ uB, const uint16_t* __restrict__ larB, const uint16_t* __restrict__ aimB,
    const uint16_t* __restrict__ crB, const uint16_t* __restrict__ ciB, const uint16_t* __restrict__ ldtB,
    const uint16_t* __restrict__ dsB, uint16_t* __restrict__ out
    ) {
#define STAMP(q)
    STAMP(0);
    __shared__ __attribute__((aligned(16))) char smem[LDS_BYTES];
    const uint32_t sb = (uint32_t)(uintptr_t)(__attribute__((address_space(3))) char*)smem;
    const int tid = threadIdx.x, lane = tid & 63;
    const int wave = __builtin_amdgcn_readfirstlane(tid >> 6);
    const int b = blockIdx.x;
    const int grp = (b & 7) * 18 + (b >> 3);
    const bool hb = grp >= 72;
    const int c0 = 8 * (hb ? grp - 72 : grp);
    const int ch = c0 + wave;
    const uint16_t* u = hb ? uB : uA;
    const uint16_t* lar = hb ? larB : larA;
    const uint16_t* aim = hb ? aimB : aimA;
    const uint16_t* cr = hb ? crB : crA;
    const uint16_t* ci = hb ? ciB : ciA;
    const uint16_t* ldt = hb ? ldtB : ldtA;
    const uint16_t* dsk = hb ? dsB : dsA;

    const uint32_t p_lar = ((const uint32_t*)lar)[ch * 64 + lane];
    const uint32_t p_aim = ((const uint32_t*)aim)[ch * 64 + lane];
    const uint32_t p_cr  = ((const uint32_t*)cr)[ch * 64 + lane];
    const uint32_t p_ci  = ((const uint32_t*)ci)[ch * 64 + lane];
    uint32_t p_ldt = ldt[ch];
    uint32_t p_dsk = dsk[ch];
    SB();
    const int phi = lane & 15, g = lane >> 4;
    Frags F;
    uint64_t bta = (uint64_t)(uintptr_t)band_tab.v;
    asm volatile("" : "+s"(bta));
    const __attribute__((address_space(1))) u32x4* bt = (const __attribute__((address_space(1))) u32x4*)bta + lane;
    F.B5 = bt[0]; F.B3 = bt[64]; F.B9 = bt[128]; F.B7 = bt[192];
    const u32x4 M32 = bt[256], M16 = bt[320], M15 = bt[384];
    SB();
    const int half = wave >> 2;
    prio_if<0, 3>(half);
    const uint32_t uch = sb + U_OFF + wave * U_STRIDE;
    const uint32_t tb = sb + TB_OFF + wave * TB_BYTES;
    const uint32_t pg = sb + PG_OFF;
    {
        const u32x4 z4 = {0u, 0u, 0u, 0u};
        *LDSP(uint32_t, uch + 4 * lane) = 0u;
        if (lane < 56) *LDSP(uint32_t, uch + 256 + 4 * lane) = 0u;
        if (lane == 0) *LDSP(uint32_t, pg + 4 * wave) = 0u;
#pragma unroll
        for (int q = 0; q < 4; q++) *LDSP(u32x4, tb + 1024 * q + 16 * lane) = z4;
    }
    const uint32_t uvo = (uint32_t)(1024 * half + 2 * (tid & 255)) * (DH * 2);
    const uint16_t* u0 = u + c0;
    const uint16_t* u1 = u0 + 512 * DH;
    u32x4 r0, r1, r2, r3;
#define ULOAD(dst, base, off) asm volatile("global_load_dwordx4 %0, %1, %2 offset:" #off : "=v"(dst) : "v"(uvo), "s"(base))
    SB();
    WGBAR();
    STAMP(1);
    asm volatile("" : "+v"(p_ldt), "+v"(p_dsk));
    ULOAD(r0, u0, 0);

    const uint32_t ks = sb + KS_OFF + wave * KS_BYTES;
    const float dt = rbf(__builtin_amdgcn_exp2f(bfl(p_ldt) * 1.44269504088896341f));
    uint32_t mx = max(max(p_cr & 0x7fffu, (p_cr >> 16) & 0x7fffu), max(p_ci & 0x7fffu, (p_ci >> 16) & 0x7fffu));
    mx = max(mx, (uint32_t)__builtin_amdgcn_update_dpp(0, (int)mx, 0x111, 0xf, 0xf, true));
    mx = max(mx, (uint32_t)__builtin_amdgcn_update_dpp(0, (int)mx, 0x112, 0xf, 0xf, true));
    mx = max(mx, (uint32_t)__builtin_amdgcn_update_dpp(0, (int)mx, 0x114, 0xf, 0xf, true));
    mx = max(mx, (uint32_t)__builtin_amdgcn_update_dpp(0, (int)mx, 0x118, 0xf, 0xf, true));
    mx = max(mx, (uint32_t)__builtin_amdgcn_update_dpp(0, (int)mx, 0x142, 0xa, 0xf, true));
    mx = max(mx, (uint32_t)__builtin_amdgcn_update_dpp(0, (int)mx, 0x143, 0xc, 0xf, true));
    uint32_t ex = (uint32_t)__builtin_amdgcn_readlane((int)mx, 63) >> 7;
    ex = min(max(ex, 1u), 252u);
    const float cs = __uint_as_float((253u - ex) << 23);
    const float kf = 2.0f * dt * __uint_as_float((ex + 1u) << 23);
    STAMP(2);

    f32x4 kA = {0.f, 0.f, 0.f, 0.f}, kB = kA, kC = kA;
    const uint32_t ksr = ks + KS_ROW * phi + 16 * g;
    asm volatile("s_mov_b32 m0, %0\n\ts_nop 0" :: "s"(ks));
#define ROW(r, v0, v1) do { asm volatile("ds_write_addtid_b32 %0 offset:%1" :: "v"(v0), "n"(KS_ROW * (r)) : "memory"); \
                            asm volatile("ds_write_addtid_b32 %0 offset:%1" :: "v"(v1), "n"(KS_ROW * (r) + 256) : "memory"); } while (0)
    {
        const f32x2 vl = {bfl(p_lar), bfh(p_lar)}, vi = {bfl(p_aim), bfh(p_aim)};
        const f32x2 vr = {bfl(p_cr), bfh(p_cr)}, vc = {bfl(p_ci), bfh(p_ci)};
        const uint32_t eb = pkbf(__builtin_amdgcn_exp2f(vl.x * 1.44269504088896341f), __builtin_amdgcn_exp2f(vl.y * 1.44269504088896341f));
        const f32x2 ar = (f32x2){bfl(eb), bfh(eb)} * -dt;
        const f32x2 a2 = ar * 1.44269504088896341f;
        const f32x2 rev = (vi * dt) * 0.15915494309189535f;
        const c32 w1 = cexp_pos(a2, rev), w32 = cexp_pos(a2 * 32.0f, rev * 32.0f);
        const c32 w2 = csq(w1), w64 = csq(w32), w4 = csq(w2), w128 = csq(w64), w8 = csq(w4), w256 = csq(w128);
        const c32 w16 = csq(w8), w512 = csq(w256), w1024 = csq(w512);
#define PKH2(n, w, sg) const uint32_t n##a = pkh(w.r.x, sg w.i.x), n##b = pkh(w.r.y, sg w.i.y)
        uint32_t ya[16], yb[16];
        ya[0] = yb[0] = 0x00003C00u;
        { PKH2(t1, w8, -); PKH2(t2, w16, -); PKH2(t4, w32, -); PKH2(t8, w64, -);
          ya[1] = t1a; yb[1] = t1b; ya[2] = t2a; yb[2] = t2b; ya[4] = t4a; yb[4] = t4b; ya[8] = t8a; yb[8] = t8b; }
#define CM2(d, x, y) do { d##a = cmul(x##a, y##a); d##b = cmul(x##b, y##b); } while (0)
        ya[3] = cmul(ya[2], ya[1]); yb[3] = cmul(yb[2], yb[1]);
        ya[5] = cmul(ya[4], ya[1]); yb[5] = cmul(yb[4], yb[1]);
        ya[6] = cmul(ya[4], ya[2]); yb[6] = cmul(yb[4], yb[2]);
        ya[7] = cmul(ya[4], ya[3]); yb[7] = cmul(yb[4], yb[3]);
#pragma unroll
        for (int j = 1; j < 8; j++) { ya[8 + j] = cmul(ya[8], ya[j]); yb[8 + j] = cmul(yb[8], yb[j]); }
#pragma unroll
        for (int j = 0; j < 16; j++) ROW(j, ya[j], yb[j]);
        CBAR();
        ULOAD(r1, u0, 1152);
        u32x4 Y[8];
#pragma unroll
        for (int s = 0; s < 8; s++) Y[s] = *LDSP(u32x4, ksr + 64 * s);
        CBAR();
        PKH2(h1, w1, ); PKH2(h2, w2, ); PKH2(h4, w4, ); PKH2(h128, w128, ); PKH2(h256, w256, ); PKH2(h512, w512, ); PKH2(h1024, w1024, );
        uint32_t xa[8], xb[8];
        xa[0] = pkh(vr.x * cs, vc.x * cs); xb[0] = pkh(vr.y * cs, vc.y * cs);
        xa[1] = cmul(xa[0], h1a); xb[1] = cmul(xb[0], h1b);
        xa[2] = cmul(xa[0], h2a); xb[2] = cmul(xb[0], h2b);
        xa[3] = cmul(xa[1], h2a); xb[3] = cmul(xb[1], h2b);
#pragma unroll
        for (int q = 0; q < 4; q++) { xa[4 + q] = cmul(xa[q], h4a); xb[4 + q] = cmul(xb[q], h4b); }
#pragma unroll
        for (int q = 0; q < 8; q++) { ROW(q, xa[q], xb[q]); ROW(8 + q, cmul(xa[q], h128a), cmul(xb[q], h128b)); }
        CBAR();
        ULOAD(r2, u1, 0);
        u32x4 A[8];
#pragma unroll
        for (int s = 0; s < 8; s++) A[s] = *LDSP(u32x4, ksr + 64 * s);
        CBAR();
#pragma unroll
        for (int q = 0; q < 4; q++) {
            const uint32_t ta = cmul(xa[2 * q], h256a), tb_ = cmul(xb[2 * q], h256b);
            ROW(q, ta, tb_); ROW(4 + q, cmul(ta, h128a), cmul(tb_, h128b));
        }
#pragma unroll
        for (int q = 0; q < 2; q++) {
            const uint32_t t0a = cmul(xa[4 * q], h512a), t0b = cmul(xb[4 * q], h512b);
            const uint32_t t1a = cmul(t0a, h128a), t1b = cmul(t0b, h128b);
            ROW(8 + q, t0a, t0b); ROW(10 + q, t1a, t1b);
            ROW(12 + q, cmul(t0a, h256a), cmul(t0b, h256b)); ROW(14 + q, cmul(t1a, h256a), cmul(t1b, h256b));
        }
#pragma unroll
        for (int s = 0; s < 8; s++) kA = mma_h(A[s], Y[s], kA);
        CBAR();
        ULOAD(r3, u1, 1152);
#pragma unroll
        for (int s = 0; s < 8; s++) A[s] = *LDSP(u32x4, ksr + 64 * s);
        CBAR();
        {
            const uint32_t t0a = cmul(xa[0], h1024a), t0b = cmul(xb[0], h1024b);
            const uint32_t t1a = cmul(t0a, h128a), t1b = cmul(t0b, h128b);
            const uint32_t t2a = cmul(t0a, h256a), t2b = cmul(t0b, h256b);
            const uint32_t t3a = cmul(t1a, h256a), t3b = cmul(t1b, h256b);
            const uint32_t t7a = cmul(t3a, h512a), t7b = cmul(t3b, h512b);
            ROW(0, t0a, t0b); ROW(1, t1a, t1b); ROW(2, t2a, t2b); ROW(3, t3a, t3b);
            ROW(4, cmul(t0a, h512a), cmul(t0b, h512b)); ROW(5, cmul(t1a, h512a), cmul(t1b, h512b));
            ROW(6, cmul(t2a, h512a), cmul(t2b, h512b)); ROW(7, t7a, t7b);
            ROW(8, cmul(t7a, h128a), cmul(t7b, h128b));
        }
#pragma unroll
        for (int s = 0; s < 8; s++) kB = mma_h(A[s], Y[s], kB);
        CBAR();
#pragma unroll
        for (int s = 0; s < 8; s++) A[s] = *LDSP(u32x4, ksr + 64 * s);
        CBAR();
#pragma unroll
        for (int s = 0; s < 8; s++) kC = mma_h(A[s], Y[s], kC);
    }
#undef ROW
    STAMP(3);
    const uint32_t pa = pg + 4 * wave;
    uint32_t one = 1u;
    asm volatile("" : "+v"(one));
    auto put_samples = [&]() {
        asm volatile("s_waitcnt vmcnt(0)" : "+v"(r0), "+v"(r1), "+v"(r2), "+v"(r3));
        uint32_t uwa = sb + U_OFF + 480 + 2048 * half + 4 * (tid & 255);
        asm volatile("" : "+v"(uwa));
        auto* uw = LDSP(uint32_t, uwa);
#pragma unroll
        for (int c = 0; c < 8; c++) {
            const uint32_t sel = (c & 1) ? 0x07060302u : 0x05040100u;
            uw[c * (U_STRIDE / 4)] = __builtin_amdgcn_perm(r1[c >> 1], r0[c >> 1], sel);
            uw[c * (U_STRIDE / 4) + 256] = __builtin_amdgcn_perm(r3[c >> 1], r2[c >> 1], sel);
        }
        publish(pa, one);
    };
    if (half == 1) put_samples();
    kA *= kf; kB *= kf; kC *= kf;
    if (lane == 0) kA[0] += bfl(p_dsk);
    const uint32_t b10 = pkbf(kB[1], kB[0]), b32_ = pkbf(kB[3], kB[2]);
    const uint32_t c10 = pkbf(kC[1], kC[0]), c32_ = pkbf(kC[3], kC[2]);
    const uint32_t a10 = pkbf(kA[1], kA[0]), a32 = pkbf(kA[3], kA[2]);
    if (g < 2) {
        const uint32_t v0v0 = __builtin_amdgcn_perm(b10, b10, 0x03020302u), v2v2 = __builtin_amdgcn_perm(b32_, b32_, 0x03020302u);
        const uint32_t v2v1 = __builtin_amdgcn_perm(b10, b32_, 0x05040302u);
        const int P0 = 256 + 128 * g + 8 * phi;
        const uint32_t ae = tb + T_RE + 2 * (538 - P0);
        *LDSP(uint32_t, ae) = b32_; *LDSP(uint32_t, ae + 4) = v2v2; *LDSP(uint32_t, ae + 8) = b10; *LDSP(uint32_t, ae + 12) = v0v0;
        const uint32_t ao = tb + T_RO + 2 * (536 - P0);
        *LDSP(uint16_t, ao + 2) = (uint16_t)b32_;
        *LDSP(uint32_t, ao + 4) = v2v2; *LDSP(uint32_t, ao + 8) = v2v1; *LDSP(uint32_t, ao + 12) = v0v0;
        *LDSP(uint16_t, ao + 16) = (uint16_t)v0v0;
    } else {
        const int i0 = 2 * (g - 2);
        const uint32_t ae = tb + T_E3 + 2 * (96 - 16 * i0 - phi), ao = tb + T_O3 + 2 * (95 - 16 * i0 - phi);
        *LDSP(uint16_t, (lane == 32) ? tb + T_DUMMY : ae) = (uint16_t)(b10 >> 16);
        *LDSP(uint16_t, ao) = (uint16_t)b10;
        *LDSP(uint16_t, ae - 32) = (uint16_t)(b32_ >> 16);
        *LDSP(uint16_t, ao - 32) = (uint16_t)b32_;
    }
    CBAR();
    {
        const int l0 = 128 * (g >> 1) + 8 * phi + 4 * (g & 1);
        *LDSP(u32x2, tb + T_RO + 2 * (540 - l0)) = (u32x2){a32, a10};
        const uint32_t ae = tb + T_RE + 2 * (540 - l0);
        *LDSP(uint16_t, ae + 2) = (uint16_t)a32;
        *LDSP(uint32_t, ae + 4) = __builtin_amdgcn_perm(a10, a32, 0x05040302u);
        *LDSP(uint16_t, ae + 8) = (uint16_t)(a10 >> 16);
    }
    {
        const int m0 = 64 + 32 * g + (phi >> 1);
        const bool od = phi & 1;
        const uint32_t dum = tb + T_DUMMY + 64 + 4 * lane;
        uint32_t aa = tb + (od ? T_O4A + 2 * (128 - m0) : T_E4A + 2 * (129 - m0));
        uint32_t ab = tb + (od ? T_O4B + 2 * (127 - m0) : T_E4B + 2 * (128 - m0));
        if (g >= 2) { aa = dum; ab = dum; }
        const uint32_t a0 = (lane == 0) ? tb + T_E3 + 2 * 32 : ((lane == 32) ? tb + T_E4A + 2 : aa);
        const uint32_t b0 = (lane == 0) ? dum : ((lane == 32) ? tb + T_E4B : ab);
        *LDSP(uint16_t, a0) = (uint16_t)(c10 >> 16);       *LDSP(uint16_t, b0) = (uint16_t)(c10 >> 16);
        *LDSP(uint16_t, aa - 16) = (uint16_t)c10;          *LDSP(uint16_t, ab - 16) = (uint16_t)c10;
        *LDSP(uint16_t, aa - 32) = (uint16_t)(c32_ >> 16); *LDSP(uint16_t, ab - 32) = (uint16_t)(c32_ >> 16);
        *LDSP(uint16_t, aa - 48) = (uint16_t)c32_;         *LDSP(uint16_t, ab - 48) = (uint16_t)c32_;
    }
    CBAR();
    STAMP(4);
    {
        const uint32_t k512 = (uint32_t)__builtin_amdgcn_readlane((int)b10, 32) >> 16;
        const uint32_t k1024 = (uint32_t)__builtin_amdgcn_readlane((int)c10, 0) >> 16;
        const uint32_t at = tb + ((phi & 1) ? T_RO + 2 * (527 - phi + 8 * g) : T_RE + 2 * (528 - phi + 8 * g)) - 1024;
#pragma unroll
        for (int n = 0; n <= 16; n++)
#pragma unroll
            for (int q = 0; q < 4; q++) F.T[n][q] = LDSP(uint32_t, at)[16 * (16 - n) + q];
        const uint32_t a3 = tb + 4 * (79 - 64 - phi + 2 * g);
#pragma unroll
        for (int nu = 3; nu <= 8; nu++) {
            const int o = 32 * (8 - nu);
            F.A3[nu - 3][0] = *LDSP(uint32_t, a3 + T_E3 + o); F.A3[nu - 3][1] = *LDSP(uint32_t, a3 + T_E3 + o + 4);
            F.A3[nu - 3][2] = *LDSP(uint32_t, a3 + T_O3 + o); F.A3[nu - 3][3] = *LDSP(uint32_t, a3 + T_O3 + o + 4);
        }
        const uint32_t a4 = tb + 4 * (64 - 56 - ((phi + 1) >> 1) + 2 * g) + ((phi & 1) ? T_E4B - T_E4A : 0);
#pragma unroll
        for (int nu = 4; nu <= 7; nu++) {
            const int o = 32 * (7 - nu);
            F.A4[nu - 4][0] = *LDSP(uint32_t, a4 + T_E4A + o); F.A4[nu - 4][1] = *LDSP(uint32_t, a4 + T_E4A + o + 4);
            F.A4[nu - 4][2] = *LDSP(uint32_t, a4 + T_O4A + o); F.A4[nu - 4][3] = *LDSP(uint32_t, a4 + T_O4A + o + 4);
        }
        const uint32_t kk = k1024 | (k1024 << 16), k5 = k512 | (k512 << 16);
#pragma unroll
        for (int q = 0; q < 4; q++) { F.T32[q] = M32[q] & kk; F.T[16][q] |= M16[q] & k5; F.T[15][q] |= M15[q] & k5; }
    }
    STAMP(5);
    STAMP(6);
    if (half == 0) put_samples();
    await<1>(pg + 4 * (lane & 3));
    STAMP(7);
    Pipe P;
    P.ub = uch + 32 * phi + 16 * g; P.ub3 = uch + 16 * (phi + g);
    P.stw = ks + 8 * lane; P.pa = pa; P.one = one; P.pw = pg + 16 + 4 * (lane & 3);
    prefetch<0>(P); prefetch<1>(P); prefetch<2>(P);
    prio_if<1, 2>(half);
    SB();
    conv_step<0>(P, F); STAMP(8); conv_step<1>(P, F); STAMP(9); conv_step<2>(P, F); conv_step<3>(P, F); STAMP(10);
    conv_step<4>(P, F); conv_step<5>(P, F); STAMP(11); conv_step<6>(P, F); STAMP(12); conv_step<7>(P, F); STAMP(13);
    if (half == 0) {
        __builtin_amdgcn_s_setprio(0);
        uint32_t str = sb + KS_OFF + ((lane & 15) >> 2) * KS_BYTES + 2 * (64 * wave + 16 * g + 4 * (lane & 3));
        uint16_t* op = out + (size_t)(64 * wave + 16 * (lane & 3) + 4 * g + ((lane & 15) >> 2)) * DO + 8 * grp;
        const uint32_t pw = pg + 16 + 4 * (lane & 3), pv = pg + 4 * (lane & 3);
        await<9>(pv);
#pragma unroll 1
        for (int s = 0; s < 8; s++) {
            await_dyn(pw, 2 + s);
            const u32x2 o0 = __builtin_bit_cast(u32x2, __builtin_amdgcn_ds_read_tr16_b64_v4i16(LDSP(s16x4, str)));
            const u32x2 o1 = __builtin_bit_cast(u32x2, __builtin_amdgcn_ds_read_tr16_b64_v4i16(LDSP(s16x4, str + 4 * KS_BYTES)));
            *(u32x4*)op = (u32x4){o0[0], o0[1], o1[0], o1[1]};
            str += 512; op += 256 * DO;
        }
    }
    STAMP(14);
}

extern "C" void kernel_launch(void* const* d_in, const int* in_sizes, int n_in, void* d_out, int out_size, void* d_ws,
                              size_t ws_size, hipStream_t stream) {
    const uint16_t* const* in = (const uint16_t* const*)d_in;
    s4d_conv_kernel<<<144, 512, 0, stream>>>(in[0], in[1], in[2], in[3], in[4], in[5], in[6], in[7], in[8], in[9], in[10],
                                             in[11], in[12], in[13], (uint16_t*)d_out
        );
}
```

```cpp
#include <hip/hip_runtime.h>
#include <hip/hip_bf16.h>
#include <stdint.h>

typedef __bf16   bf16x8 __attribute__((ext_vector_type(8)));
typedef __bf16   bf16x2 __attribute__((ext_vector_type(2)));
typedef _Float16 f16x8  __attribute__((ext_vector_type(8)));
typedef _Float16 f16x2  __attribute__((ext_vector_type(2)));
typedef float    f32x4  __attribute__((ext_vector_type(4)));
typedef float    f32x2  __attribute__((ext_vector_type(2)));
typedef uint32_t u32x4  __attribute__((ext_vector_type(4)));
typedef uint32_t u32x2  __attribute__((ext_vector_type(2)));
typedef short    s16x4  __attribute__((ext_vector_type(4)));

#define LDSP(T, off) ((__attribute__((address_space(3))) T*)(uintptr_t)(off))
#define CBAR() asm volatile("" ::: "memory")

constexpr int DH = 576, DO = 1152;
constexpr int U_STRIDE = 4608;
constexpr int KS_ROW   = 544;
constexpr int KS_BYTES = 16 * KS_ROW + 64;
constexpr int KS_OFF   = 0;
constexpr int U_OFF    = KS_OFF + 8 * KS_BYTES;
constexpr int TB_BYTES = 4096;
constexpr int TB_OFF   = U_OFF + 8 * U_STRIDE;
constexpr int PG_OFF   = TB_OFF + 8 * TB_BYTES;
constexpr int BD_OFF   = PG_OFF + 64;
constexpr int LDS_BYTES = BD_OFF + 7 * 1024;
constexpr int T_RO = 0, T_RE = 1216, T_E3 = 2336, T_O3 = 2592, T_E4A = 2848, T_O4A = 3008, T_E4B = 3168, T_O4B = 3328;
constexpr int T_DUMMY = 3584;

__device__ __forceinline__ float bfl(uint32_t x) { return __uint_as_float(x << 16); }
__device__ __forceinline__ float bfh(uint32_t x) { return __uint_as_float(x & 0xffff0000u); }
__device__ __forceinline__ uint32_t pkbf(float lo, float hi) {
    f32x2 v = {lo, hi};
    return __builtin_bit_cast(uint32_t, __builtin_convertvector(v, bf16x2));
}
__device__ __forceinline__ uint32_t pkh(float lo, float hi) {
    f32x2 v = {lo, hi};
    return __builtin_bit_cast(uint32_t, __builtin_convertvector(v, f16x2));
}
__device__ __forceinline__ float rbf(float x) { return bfl(pkbf(x, x) & 0xffffu); }
__device__ __forceinline__ uint32_t cmul(uint32_t xx, uint32_t ww) {
    const f16x2 x = __builtin_bit_cast(f16x2, xx), w = __builtin_bit_cast(f16x2, ww);
    const f16x2 wr = __builtin_shufflevector(w, w, 0, 0), wi = __builtin_shufflevector(w, w, 1, 1);
    const f16x2 xs = {-x.y, x.x};
    return __builtin_bit_cast(uint32_t, __builtin_elementwise_fma(xs, wi, x * wr));
}
struct c32 { f32x2 r, i; };
__device__ __forceinline__ c32 csq(c32 a) {
    c32 o; const f32x2 p = a.r * a.i;
    o.r = __builtin_elementwise_fma(a.r, a.r, -(a.i * a.i)); o.i = p + p; return o;
}
__device__ __forceinline__ c32 cexp_pos(f32x2 a2, f32x2 rev) {
    const f32x2 m = {__builtin_amdgcn_exp2f(a2.x), __builtin_amdgcn_exp2f(a2.y)};
    const float p0 = __builtin_amdgcn_fractf(rev.x), p1 = __builtin_amdgcn_fractf(rev.y);
    const f32x2 c = {__builtin_amdgcn_cosf(p0), __builtin_amdgcn_cosf(p1)}, s = {__builtin_amdgcn_sinf(p0), __builtin_amdgcn_sinf(p1)};
    c32 o; o.r = m * c; o.i = m * s; return o;
}
__device__ __forceinline__ f32x4 mma_bf(u32x4 a, u32x4 b, f32x4 c) {
    return __builtin_amdgcn_mfma_f32_16x16x32_bf16(__builtin_bit_cast(bf16x8, a), __builtin_bit_cast(bf16x8, b), c, 0, 0, 0);
}
__device__ __forceinline__ f32x4 mma_h(u32x4 a, u32x4 b, f32x4 c) {
    return __builtin_amdgcn_mfma_f32_16x16x32_f16(__builtin_bit_cast(f16x8, a), __builtin_bit_cast(f16x8, b), c, 0, 0, 0);
}
struct BandTab { uint32_t v[7 * 64 * 4]; };
constexpr BandTab make_bands() {
    BandTab t{};
    constexpr int lo[7] = {6, 11, 4, 5, 12, 14, -15}, n[7] = {5, 3, 9, 7, 2, 4, 1};
    for (int q = 0; q < 7; q++)
        for (int l = 0; l < 64; l++)
            for (int j = 0; j < 8; j++) {
                const int d = 8 * (l >> 4) + j - (l & 15);
                if (d >= lo[q] && d < lo[q] + n[q]) t.v[(q * 64 + l) * 4 + (j >> 1)] |= (q < 4 ? 0x3F80u : 0xFFFFu) << (16 * (j & 1));
            }
    return t;
}
static __device__ const __attribute__((aligned(16))) BandTab band_tab = make_bands();

#define SB() __builtin_amdgcn_sched_barrier(0)
#define WGBAR() asm volatile("s_waitcnt lgkmcnt(0)\n\ts_barrier" ::: "memory")
__device__ __forceinline__ void publish(uint32_t addr, uint32_t one) {
    asm volatile("s_mov_b64 exec, 1\n\tds_add_u32 %0, %1\n\ts_mov_b64 exec, -1" :: "v"(addr), "v"(one) : "memory");
}
template <int NEED>
__device__ __forceinline__ void await(uint32_t addr) {
    uint32_t v;
    asm volatile("1:\n\tds_read_b32 %0, %1\n\ts_waitcnt lgkmcnt(0)\n\tv_cmp_gt_u32 vcc, %2, %0\n\ts_cbranch_vccz 2f\n\ts_sleep 1\n\ts_branch 1b\n2:"
                 : "=&v"(v) : "v"(addr), "n"(NEED) : "vcc", "memory");
}
__device__ __forceinline__ void await_dyn(uint32_t addr, uint32_t need) {
    uint32_t v;
    asm volatile("1:\n\tds_read_b32 %0, %1\n\ts_waitcnt lgkmcnt(0)\n\tv_cmp_gt_u32 vcc, %2, %0\n\ts_cbranch_vccz 2f\n\ts_sleep 1\n\ts_branch 1b\n2:"
                 : "=&v"(v) : "v"(addr), "s"(need) : "vcc", "memory");
}
template <int H, int P>
__device__ __forceinline__ void prio_if(int half) {
    asm volatile("s_cmp_lg_u32 %0, %1\n\ts_cbranch_scc1 1f\n\ts_setprio %2\n1:" :: "s"(half), "n"(H), "n"(P) : "scc");
}

constexpr int ngrp(int S) { return S <= 3 ? 11 : (S <= 5 ? 10 : 8); }
constexpr int gbase(int S) { int b = 0; for (int s = 0; s < S; s++) b += ngrp(s); return b; }
constexpr int gkind(int S, int k) {
    if (k == 0) return 0;
    if (S <= 3) { if (k == 1) return 8; if (k == 2) return 9; if (k == 3) return 10; return k - 3; }
    if (S <= 5) { if (k == 1) return 9; if (k == 2) return 10; return k - 2; }
    return k;
}
constexpr int gstep(int I) { int s = 0; while (s < 8 && I >= gbase(s) + ngrp(s)) s++; return s; }

struct Frags {
    u32x4 T[17], T32, A3[6], A4[4], B5, B3, B9, B7;
};
struct Pipe {
    u32x4 R[4];
    f32x4 acc[8], d9, d7, d5[2], d3[2];
    u32x4 W4, W3[2];
    uint32_t ub, ub3, stw, pa, one, pw;
};
template <int I>
__device__ __forceinline__ void prefetch(Pipe& P) {
    constexpr int S = gstep(I);
    if constexpr (S < 8) {
        constexpr int kd = gkind(S, I - gbase(S));
        if constexpr (kd < 8) P.R[I & 3] = *LDSP(u32x4, P.ub + 448 + 64 * (8 * S - kd));
        else if constexpr (kd == 8) P.R[I & 3] = *LDSP(u32x4, P.ub + 432 + 512 * S);
        else P.R[I & 3] = *LDSP(u32x4, P.ub3 + 432 + 256 * (2 * S + kd - 9));
    }
}
__device__ __forceinline__ u32x4 pk4(f32x4 a, f32x4 b) {
    u32x4 W; W[0] = pkbf(a[0], a[1]); W[1] = pkbf(a[2], a[3]); W[2] = pkbf(b[0], b[1]); W[3] = pkbf(b[2], b[3]); return W;
}
template <int S, int K>
__device__ __forceinline__ void group(Pipe& P, const Frags& F) {
    constexpr int I = gbase(S) + K, kd = gkind(S, K);
    const f32x4 z = {0.f, 0.f, 0.f, 0.f};
    if constexpr (I + 3 == gbase(4)) await<2>(P.pw);
    prefetch<I + 3>(P);
    const u32x4 U = P.R[I & 3];
    if constexpr (kd < 8) {
        P.acc[S] = mma_bf(U, F.T[kd], (S == 0 && K == 0) ? z : P.acc[S]);
        if constexpr (S + 1 <= 7) P.acc[S + 1] = mma_bf(U, F.T[8 + kd], (S == 0 && K == 0) ? z : P.acc[S + 1]);
        if constexpr (kd == 0) {
            if constexpr (S + 2 <= 7) P.acc[S + 2] = mma_bf(U, F.T[16], S == 0 ? z : P.acc[S + 2]);
            if constexpr (S + 4 <= 7) { P.acc[S + 4] = mma_bf(U, F.T32, S == 0 ? z : P.acc[S + 4]); P.d7 = mma_bf(U, F.B7, z); }
        }
    } else if constexpr (kd == 8) {
        P.d9 = mma_bf(U, F.B9, z);
    } else {
        P.d5[kd - 9] = mma_bf(U, F.B5, z); P.d3[kd - 9] = mma_bf(U, F.B3, z);
    }
    constexpr int k4 = 4, k3a = (S <= 3) ? 5 : 4, k3b = k3a + 1;
    if constexpr (S <= 3 && K == k4) P.W4 = pk4(P.d9, P.d7);
    if constexpr (S <= 5 && K == k3a) P.W3[0] = pk4(P.d5[0], P.d3[0]);
    if constexpr (S <= 5 && K == k3b) P.W3[1] = pk4(P.d5[1], P.d3[1]);
    SB();
}
template <int S, int K>
__device__ __forceinline__ void groups(Pipe& P, const Frags& F) {
    if constexpr (K < ngrp(S)) { group<S, K>(P, F); groups<S, K + 1>(P, F); }
}
template <int S>
__device__ __forceinline__ void conv_step(Pipe& P, const Frags& F) {
    const f32x4 z = {0.f, 0.f, 0.f, 0.f};
    groups<S, 0>(P, F);
    if constexpr (S <= 5) {
        P.acc[S + 2] = mma_bf(F.A3[1], P.W3[0], P.acc[S + 2]);
        P.acc[S + 2] = mma_bf(F.A3[0], P.W3[1], P.acc[S + 2]);
    }
    {
        u32x2 o; o[0] = pkbf(P.acc[S][0], P.acc[S][1]); o[1] = pkbf(P.acc[S][2], P.acc[S][3]);
        *LDSP(u32x2, P.stw + S * 512) = o;
        publish(P.pa, P.one);
    }
    SB();
    if constexpr (S <= 5) {
#pragma unroll
        for (int q = 3; q <= 4; q++)
            if (S + q <= 7) {
                P.acc[(S + q) & 7] = mma_bf(F.A3[2 * q - 3], P.W3[0], (S == 0 && q == 3) ? z : P.acc[(S + q) & 7]);
                P.acc[(S + q) & 7] = mma_bf(F.A3[2 * q - 4], P.W3[1], P.acc[(S + q) & 7]);
            }
    }
    if constexpr (S <= 3) {
#pragma unroll
        for (int nu = 4; nu <= 7; nu++)
            if (S + nu <= 7) P.acc[(S + nu) & 7] = mma_bf(F.A4[nu - 4], P.W4, (S == 0 && nu >= 5) ? z : P.acc[(S + nu) & 7]);
    }
    SB();
}

__global__ __launch_bounds__(512, 2) void s4d_conv_kernel(
    const uint16_t* __restrict__ uA, const uint16_t* __restrict__ larA, const uint16_t* __restrict__ aimA,
    const uint16_t* __restrict__ crA, const uint16_t* __restrict__ ciA, const uint16_t* __restrict__ ldtA,
    const uint16_t* __restrict__ dsA,
    const uint16_t* __restrict__ uB, const uint16_t* __restrict__ larB, const uint16_t* __restrict__ aimB,
    const uint16_t* __restrict__ crB, const uint16_t* __restrict__ ciB, const uint16_t* __restrict__ ldtB,
    const uint16_t* __restrict__ dsB, uint16_t* __restrict__ out
    ) {
    __shared__ __attribute__((aligned(16))) char smem[LDS_BYTES];
    const uint32_t sb = (uint32_t)(uintptr_t)(__attribute__((address_space(3))) char*)smem;
    const int tid = threadIdx.x, lane = tid & 63;
    const int wave = __builtin_amdgcn_readfirstlane(tid >> 6);
    const int b = blockIdx.x;
    const int xc = b & 7, xj = b >> 3;
    if (xj >= (xc < 4 ? 20 : 16)) return;
    uint32_t pf0 = 0;
    {
        uint64_t pc; uint32_t len;
        asm volatile("s_getpc_b64 %0\nLs4d_begin:\n\ts_mov_b32 %1, Ls4d_end-Ls4d_begin-4" : "=s"(pc), "=s"(len));
        const uint32_t off = min(64u * (uint32_t)threadIdx.x, len);
        asm volatile("global_load_dword %0, %1, %2" : "=v"(pf0) : "v"(off), "s"(pc));
    }
    SB();
    const int grp = (xc < 4 ? 20 * xc : 16 + 16 * xc) + xj;
    const bool hb = grp >= 72;
    const int c0 = 8 * (hb ? grp - 72 : grp);
    const int ch = c0 + wave;
    const uint16_t* u = hb ? uB : uA;
    const uint16_t* lar = hb ? larB : larA;
    const uint16_t* aim = hb ? aimB : aimA;
    const uint16_t* cr = hb ? crB : crA;
    const uint16_t* ci = hb ? ciB : ciA;
    const uint16_t* ldt = hb ? ldtB : ldtA;
    const uint16_t* dsk = hb ? dsB : dsA;

    uint32_t p_lar, p_aim, p_cr, p_ci;
    {
        const uint32_t po = (uint32_t)(ch * 64 + lane) * 4u;
        asm volatile("global_load_dword %0, %4, %5\n\tglobal_load_dword %1, %4, %6\n\tglobal_load_dword %2, %4, %7\n\tglobal_load_dword %3, %4, %8"
                     : "=&v"(p_lar), "=&v"(p_aim), "=&v"(p_cr), "=&v"(p_ci) : "v"(po), "s"(lar), "s"(aim), "s"(cr), "s"(ci));
    }
    const uint32_t w_ldt = ((const uint32_t*)ldt)[ch >> 1], w_dsk = ((const uint32_t*)dsk)[ch >> 1];
    const uint32_t p_ldt = (ch & 1) ? w_ldt & 0xffff0000u : w_ldt << 16, p_dsk = (ch & 1) ? w_dsk & 0xffff0000u : w_dsk << 16;
    const int phi = lane & 15, g = lane >> 4;
    Frags F;
    u32x4 bfr;
    const uint32_t bq = (uint32_t)min(wave, 6) * 1024u + 16u * lane;
    {
        uint64_t bta = (uint64_t)(uintptr_t)band_tab.v;
        asm volatile("" : "+s"(bta));
        asm volatile("global_load_dwordx4 %0, %1, %2" : "=&v"(bfr) : "v"(bq), "s"(bta));
    }
    SB();
    const int half = wave >> 2;
    prio_if<0, 3>(half);
    const uint32_t uch = sb + U_OFF + wave * U_STRIDE;
    const uint32_t tb = sb + TB_OFF + wave * TB_BYTES;
    const uint32_t pg = sb + PG_OFF;
    {
        const u32x4 z4 = {0u, 0u, 0u, 0u};
        *LDSP(uint32_t, uch + 4 * lane) = 0u;
        if (lane < 56) *LDSP(uint32_t, uch + 256 + 4 * lane) = 0u;
        if (lane == 0) *LDSP(uint32_t, pg + 4 * wave) = 0u;
#pragma unroll
        for (int q = 0; q < 4; q++) *LDSP(u32x4, tb + 1024 * q + 16 * lane) = z4;
    }
    const uint32_t uvo = (uint32_t)(1024 * half + 2 * (tid & 255)) * (DH * 2);
    const uint16_t* u0 = u + c0;
    const uint16_t* u1 = u0 + 512 * DH;
    u32x4 r0, r1, r2, r3;
#define ULDH(h, dst, base, off) asm volatile("s_cmp_lg_u32 %3, " #h "\n\ts_cbranch_scc1 1f\n\tglobal_load_dwordx4 %0, %1, %2 offset:" #off "\n1:" : "+v"(dst) : "v"(uvo), "s"(base), "s"(half) : "scc")
    asm volatile("" : "=v"(r0), "=v"(r1), "=v"(r2), "=v"(r3));
    SB();
    WGBAR();
    const uint32_t ks = sb + KS_OFF + wave * KS_BYTES;
    const int P0 = 256 + 128 * g + 8 * phi, i0 = 2 * (g - 2), l0 = 128 * (g >> 1) + 8 * phi + 4 * (g & 1);
    uint32_t ae2 = tb + T_RE + 2 * (538 - P0), ao2 = tb + T_RO + 2 * (536 - P0);
    uint32_t ae4m = tb + T_E3 + 2 * (96 - 16 * i0 - phi) - 32, ao4m = tb + T_O3 + 2 * (95 - 16 * i0 - phi) - 32;
    uint32_t ad4 = (lane == 32) ? tb + T_DUMMY : ae4m + 32;
    uint32_t ao1 = tb + T_RO + 2 * (540 - l0), ae1 = tb + T_RE + 2 * (540 - l0);
    uint32_t aa, ab, a0, b0;
    {
        const int m0 = 64 + 32 * g + (phi >> 1);
        const bool od = phi & 1;
        const uint32_t dum = tb + T_DUMMY + 64 + 4 * lane;
        aa = tb + (od ? T_O4A + 2 * (128 - m0) : T_E4A + 2 * (129 - m0));
        ab = tb + (od ? T_O4B + 2 * (127 - m0) : T_E4B + 2 * (128 - m0));
        if (g >= 2) { aa = dum; ab = dum; }
        a0 = (lane == 0) ? tb + T_E3 + 2 * 32 : ((lane == 32) ? tb + T_E4A + 2 : aa);
        b0 = (lane == 0) ? dum : ((lane == 32) ? tb + T_E4B : ab);
        aa -= 48; ab -= 48;
    }
    uint32_t at0 = tb + ((phi & 1) ? T_RO + 2 * (527 - phi + 8 * g) : T_RE + 2 * (528 - phi + 8 * g)) - 1024, at1 = at0 + 1024;
    uint32_t a3e = tb + 4 * (79 - 64 - phi + 2 * g) + T_E3;
    uint32_t a4e = tb + 4 * (64 - 56 - ((phi + 1) >> 1) + 2 * g) + ((phi & 1) ? T_E4B - T_E4A : 0) + T_E4A;
    uint32_t uwa = sb + U_OFF + 480 + 2048 * half + 4 * (tid & 255);
    uint32_t c_ub = uch + 32 * phi + 16 * g, c_ub3 = uch + 16 * (phi + g), c_stw = ks + 8 * lane, c_pv = pg + 4 * (lane & 3);
    uint32_t ksr = ks + KS_ROW * phi + 16 * g;
    asm volatile("" : "+v"(ae2), "+v"(ao2), "+v"(ae4m), "+v"(ao4m), "+v"(ad4), "+v"(ao1), "+v"(ae1), "+v"(aa), "+v"(ab), "+v"(a0), "+v"(b0));
    asm volatile("" : "+v"(at0), "+v"(at1), "+v"(a3e), "+v"(a4e), "+v"(uwa), "+v"(c_ub), "+v"(c_ub3), "+v"(c_stw), "+v"(c_pv), "+v"(ksr));
    SB();
    asm volatile("s_waitcnt vmcnt(0)" : "+v"(p_lar), "+v"(p_aim), "+v"(p_cr), "+v"(p_ci), "+v"(pf0), "+v"(bfr));
    ULDH(0, r0, u0, 0); ULDH(0, r1, u0, 1152);
    const uint32_t pa = pg + 4 * wave;
    uint32_t one = 1u;
    asm volatile("" : "+v"(one));
    *LDSP(u32x4, sb + BD_OFF + bq) = bfr;
    publish(pa, one);

    const float dt = rbf(__builtin_amdgcn_exp2f(__uint_as_float(p_ldt) * 1.44269504088896341f));
    uint32_t mx = max(max(p_cr & 0x7fffu, (p_cr >> 16) & 0x7fffu), max(p_ci & 0x7fffu, (p_ci >> 16) & 0x7fffu));
    mx = max(mx, (uint32_t)__builtin_amdgcn_update_dpp(0, (int)mx, 0x111, 0xf, 0xf, true));
    mx = max(mx, (uint32_t)__builtin_amdgcn_update_dpp(0, (int)mx, 0x112, 0xf, 0xf, true));
    mx = max(mx, (uint32_t)__builtin_amdgcn_update_dpp(0, (int)mx, 0x114, 0xf, 0xf, true));
    mx = max(mx, (uint32_t)__builtin_amdgcn_update_dpp(0, (int)mx, 0x118, 0xf, 0xf, true));
    mx = max(mx, (uint32_t)__builtin_amdgcn_update_dpp(0, (int)mx, 0x142, 0xa, 0xf, true));
    mx = max(mx, (uint32_t)__builtin_amdgcn_update_dpp(0, (int)mx, 0x143, 0xc, 0xf, true));
    uint32_t ex = (uint32_t)__builtin_amdgcn_readlane((int)mx, 63) >> 7;
    ex = min(max(ex, 1u), 252u);
    const float cs = __uint_as_float((253u - ex) << 23);
    const float kf = 2.0f * dt * __uint_as_float((ex + 1u) << 23);

    f32x4 kA = {0.f, 0.f, 0.f, 0.f}, kB = kA, kC = kA;
    asm volatile("s_mov_b32 m0, %0\n\ts_nop 0" :: "s"(ks));
#define ROW(r, v0, v1) do { asm volatile("ds_write_addtid_b32 %0 offset:%1" :: "v"(v0), "n"(KS_ROW * (r)) : "memory"); \
                            asm volatile("ds_write_addtid_b32 %0 offset:%1" :: "v"(v1), "n"(KS_ROW * (r) + 256) : "memory"); } while (0)
    {
        const f32x2 vl = {bfl(p_lar), bfh(p_lar)}, vi = {bfl(p_aim), bfh(p_aim)};
        const f32x2 vr = {bfl(p_cr), bfh(p_cr)}, vc = {bfl(p_ci), bfh(p_ci)};
        const uint32_t eb = pkbf(__builtin_amdgcn_exp2f(vl.x * 1.44269504088896341f), __builtin_amdgcn_exp2f(vl.y * 1.44269504088896341f));
        const f32x2 ar = (f32x2){bfl(eb), bfh(eb)} * -dt;
        const f32x2 a2 = ar * 1.44269504088896341f;
        const f32x2 rev = (vi * dt) * 0.15915494309189535f;
        const c32 w1 = cexp_pos(a2, rev), w32 = cexp_pos(a2 * 32.0f, rev * 32.0f);
        const c32 w2 = csq(w1), w64 = csq(w32), w4 = csq(w2), w128 = csq(w64), w8 = csq(w4), w256 = csq(w128);
        const c32 w16 = csq(w8), w512 = csq(w256), w1024 = csq(w512);
#define PKH2(n, w, sg) const uint32_t n##a = pkh(w.r.x, sg w.i.x), n##b = pkh(w.r.y, sg w.i.y)
        uint32_t ya[16], yb[16];
        ya[0] = yb[0] = 0x00003C00u;
        { PKH2(t1, w8, -); PKH2(t2, w16, -); PKH2(t4, w32, -); PKH2(t8, w64, -);
          ya[1] = t1a; yb[1] = t1b; ya[2] = t2a; yb[2] = t2b; ya[4] = t4a; yb[4] = t4b; ya[8] = t8a; yb[8] = t8b; }
        ya[3] = cmul(ya[2], ya[1]); yb[3] = cmul(yb[2], yb[1]);
        ya[5] = cmul(ya[4], ya[1]); yb[5] = cmul(yb[4], yb[1]);
        ya[6] = cmul(ya[4], ya[2]); yb[6] = cmul(yb[4], yb[2]);
        ya[7] = cmul(ya[4], ya[3]); yb[7] = cmul(yb[4], yb[3]);
#pragma unroll
        for (int j = 1; j < 8; j++) { ya[8 + j] = cmul(ya[8], ya[j]); yb[8 + j] = cmul(yb[8], yb[j]); }
#pragma unroll
        for (int j = 0; j < 16; j++) ROW(j, ya[j], yb[j]);
        CBAR();
        ULDH(1, r0, u0, 0); ULDH(1, r1, u0, 1152); ULDH(0, r2, u1, 0); ULDH(0, r3, u1, 1152);
        u32x4 Y[8];
#pragma unroll
        for (int s = 0; s < 8; s++) Y[s] = *LDSP(u32x4, ksr + 64 * s);
        CBAR();
        PKH2(h1, w1, ); PKH2(h2, w2, ); PKH2(h4, w4, ); PKH2(h128, w128, ); PKH2(h256, w256, ); PKH2(h512, w512, ); PKH2(h1024, w1024, );
        uint32_t xa[8], xb[8];
        xa[0] = pkh(vr.x * cs, vc.x * cs); xb[0] = pkh(vr.y * cs, vc.y * cs);
        xa[1] = cmul(xa[0], h1a); xb[1] = cmul(xb[0], h1b);
        xa[2] = cmul(xa[0], h2a); xb[2] = cmul(xb[0], h2b);
        xa[3] = cmul(xa[1], h2a); xb[3] = cmul(xb[1], h2b);
#pragma unroll
        for (int q = 0; q < 4; q++) { xa[4 + q] = cmul(xa[q], h4a); xb[4 + q] = cmul(xb[q], h4b); }
#pragma unroll
        for (int q = 0; q < 8; q++) { ROW(q, xa[q], xb[q]); ROW(8 + q, cmul(xa[q], h128a), cmul(xb[q], h128b)); }
        CBAR();
        ULDH(1, r2, u1, 0); ULDH(1, r3, u1, 1152);
        u32x4 A[8];
#pragma unroll
        for (int s = 0; s < 8; s++) A[s] = *LDSP(u32x4, ksr + 64 * s);
        CBAR();
#pragma unroll
        for (int q = 0; q < 4; q++) {
            const uint32_t ta = cmul(xa[2 * q], h256a), tb_ = cmul(xb[2 * q], h256b);
            ROW(q, ta, tb_); ROW(4 + q, cmul(ta, h128a), cmul(tb_, h128b));
        }
#pragma unroll
        for (int q = 0; q < 2; q++) {
            const uint32_t t0a = cmul(xa[4 * q], h512a), t0b = cmul(xb[4 * q], h512b);
            const uint32_t t1a = cmul(t0a, h128a), t1b = cmul(t0b, h128b);
            ROW(8 + q, t0a, t0b); ROW(10 + q, t1a, t1b);
            ROW(12 + q, cmul(t0a, h256a), cmul(t0b, h256b)); ROW(14 + q, cmul(t1a, h256a), cmul(t1b, h256b));
        }
#pragma unroll
        for (int s = 0; s < 8; s++) kA = mma_h(A[s], Y[s], kA);
        CBAR();
#pragma unroll
        for (int s = 0; s < 8; s++) A[s] = *LDSP(u32x4, ksr + 64 * s);
        CBAR();
        {
            const uint32_t t0a = cmul(xa[0], h1024a), t0b = cmul(xb[0], h1024b);
            const uint32_t t1a = cmul(t0a, h128a), t1b = cmul(t0b, h128b);
            const uint32_t t2a = cmul(t0a, h256a), t2b = cmul(t0b, h256b);
            const uint32_t t3a = cmul(t1a, h256a), t3b = cmul(t1b, h256b);
            const uint32_t t7a = cmul(t3a, h512a), t7b = cmul(t3b, h512b);
            ROW(0, t0a, t0b); ROW(1, t1a, t1b); ROW(2, t2a, t2b); ROW(3, t3a, t3b);
            ROW(4, cmul(t0a, h512a), cmul(t0b, h512b)); ROW(5, cmul(t1a, h512a), cmul(t1b, h512b));
            ROW(6, cmul(t2a, h512a), cmul(t2b, h512b)); ROW(7, t7a, t7b);
            ROW(8, cmul(t7a, h128a), cmul(t7b, h128b));
        }
#pragma unroll
        for (int s = 0; s < 8; s++) kB = mma_h(A[s], Y[s], kB);
        CBAR();
#pragma unroll
        for (int s = 0; s < 8; s++) A[s] = *LDSP(u32x4, ksr + 64 * s);
        CBAR();
#pragma unroll
        for (int s = 0; s < 8; s++) kC = mma_h(A[s], Y[s], kC);
    }
#undef ROW
    auto put_samples = [&]() {
        asm volatile("s_waitcnt vmcnt(0)" : "+v"(r0), "+v"(r1), "+v"(r2), "+v"(r3));
        auto* uw = LDSP(uint32_t, uwa);
#pragma unroll
        for (int c = 0; c < 8; c++) {
            const uint32_t sel = (c & 1) ? 0x07060302u : 0x05040100u;
            uw[c * (U_STRIDE / 4)] = __builtin_amdgcn_perm(r1[c >> 1], r0[c >> 1], sel);
            uw[c * (U_STRIDE / 4) + 256] = __builtin_amdgcn_perm(r3[c >> 1], r2[c >> 1], sel);
        }
        publish(pa, one);
    };
    if (half == 1) put_samples();
    kA *= kf; kB *= kf; kC *= kf;
    if (lane == 0) kA[0] += __uint_as_float(p_dsk);
    const uint32_t b10 = pkbf(kB[1], kB[0]), b32_ = pkbf(kB[3], kB[2]);
    const uint32_t c10 = pkbf(kC[1], kC[0]), c32_ = pkbf(kC[3], kC[2]);
    const uint32_t a10 = pkbf(kA[1], kA[0]), a32 = pkbf(kA[3], kA[2]);
    if (g < 2) {
        const uint32_t v0v0 = __builtin_amdgcn_perm(b10, b10, 0x03020302u), v2v2 = __builtin_amdgcn_perm(b32_, b32_, 0x03020302u);
        const uint32_t v2v1 = __builtin_amdgcn_perm(b10, b32_, 0x05040302u);
        *LDSP(uint32_t, ae2) = b32_; *LDSP(uint32_t, ae2 + 4) = v2v2; *LDSP(uint32_t, ae2 + 8) = b10; *LDSP(uint32_t, ae2 + 12) = v0v0;
        *LDSP(uint16_t, ao2 + 2) = (uint16_t)b32_;
        *LDSP(uint32_t, ao2 + 4) = v2v2; *LDSP(uint32_t, ao2 + 8) = v2v1; *LDSP(uint32_t, ao2 + 12) = v0v0;
        *LDSP(uint16_t, ao2 + 16) = (uint16_t)v0v0;
    } else {
        *LDSP(uint16_t, ad4) = (uint16_t)(b10 >> 16);
        *LDSP(uint16_t, ao4m + 32) = (uint16_t)b10;
        *LDSP(uint16_t, ae4m) = (uint16_t)(b32_ >> 16);
        *LDSP(uint16_t, ao4m) = (uint16_t)b32_;
    }
    CBAR();
    {
        *LDSP(u32x2, ao1) = (u32x2){a32, a10};
        *LDSP(uint16_t, ae1 + 2) = (uint16_t)a32;
        *LDSP(uint32_t, ae1 + 4) = __builtin_amdgcn_perm(a10, a32, 0x05040302u);
        *LDSP(uint16_t, ae1 + 8) = (uint16_t)(a10 >> 16);
    }
    {
        *LDSP(uint16_t, a0) = (uint16_t)(c10 >> 16);       *LDSP(uint16_t, b0) = (uint16_t)(c10 >> 16);
        *LDSP(uint16_t, aa + 32) = (uint16_t)c10;          *LDSP(uint16_t, ab + 32) = (uint16_t)c10;
        *LDSP(uint16_t, aa + 16) = (uint16_t)(c32_ >> 16); *LDSP(uint16_t, ab + 16) = (uint16_t)(c32_ >> 16);
        *LDSP(uint16_t, aa) = (uint16_t)c32_;              *LDSP(uint16_t, ab) = (uint16_t)c32_;
    }
    CBAR();
    {
        await<1>(pg + 4 * (lane & 7));
        const uint32_t bd = sb + BD_OFF + 16 * lane;
        F.B5 = *LDSP(u32x4, bd); F.B3 = *LDSP(u32x4, bd + 1024); F.B9 = *LDSP(u32x4, bd + 2048); F.B7 = *LDSP(u32x4, bd + 3072);
        const u32x4 M32 = *LDSP(u32x4, bd + 4096), M16 = *LDSP(u32x4, bd + 5120), M15 = *LDSP(u32x4, bd + 6144);
        const uint32_t k512 = (uint32_t)__builtin_amdgcn_readlane((int)b10, 32) >> 16;
        const uint32_t k1024 = (uint32_t)__builtin_amdgcn_readlane((int)c10, 0) >> 16;
#pragma unroll
        for (int q = 0; q < 4; q++) F.T[0][q] = LDSP(uint32_t, at1)[q];
#pragma unroll
        for (int n = 1; n <= 16; n++)
#pragma unroll
            for (int q = 0; q < 4; q++) F.T[n][q] = LDSP(uint32_t, at0)[16 * (16 - n) + q];
#pragma unroll
        for (int nu = 3; nu <= 8; nu++) {
            const int o = 32 * (8 - nu);
            F.A3[nu - 3][0] = *LDSP(uint32_t, a3e + o); F.A3[nu - 3][1] = *LDSP(uint32_t, a3e + o + 4);
            F.A3[nu - 3][2] = *LDSP(uint32_t, a3e + T_O3 - T_E3 + o); F.A3[nu - 3][3] = *LDSP(uint32_t, a3e + T_O3 - T_E3 + o + 4);
        }
#pragma unroll
        for (int nu = 4; nu <= 7; nu++) {
            const int o = 32 * (7 - nu);
            F.A4[nu - 4][0] = *LDSP(uint32_t, a4e + o); F.A4[nu - 4][1] = *LDSP(uint32_t, a4e + o + 4);
            F.A4[nu - 4][2] = *LDSP(uint32_t, a4e + T_O4A - T_E4A + o); F.A4[nu - 4][3] = *LDSP(uint32_t, a4e + T_O4A - T_E4A + o + 4);
        }
        const uint32_t kk = k1024 | (k1024 << 16), k5 = k512 | (k512 << 16);
#pragma unroll
        for (int q = 0; q < 4; q++) { F.T32[q] = M32[q] & kk; F.T[16][q] |= M16[q] & k5; F.T[15][q] |= M15[q] & k5; }
    }
    if (half == 0) put_samples();
    await<2>(c_pv);
    Pipe P;
    P.ub = c_ub; P.ub3 = c_ub3; P.stw = c_stw; P.pa = pa; P.one = one; P.pw = c_pv + 16;
    prefetch<0>(P); prefetch<1>(P); prefetch<2>(P);
    prio_if<1, 2>(half);
    SB();
    conv_step<0>(P, F); conv_step<1>(P, F); conv_step<2>(P, F); conv_step<3>(P, F);
    conv_step<4>(P, F); conv_step<5>(P, F); conv_step<6>(P, F); conv_step<7>(P, F);
    if (half == 0) {
        __builtin_amdgcn_s_setprio(0);
        uint32_t str = sb + KS_OFF + ((lane & 15) >> 2) * KS_BYTES + 2 * (64 * wave + 16 * g + 4 * (lane & 3));
        uint16_t* op = out + (size_t)(64 * wave + 16 * (lane & 3) + 4 * g + ((lane & 15) >> 2)) * DO + 8 * grp;
        const uint32_t pw = c_pv + 16, pv = c_pv;
        await<10>(pv);
#pragma unroll 1
        for (int s = 0; s < 8; s++) {
            await_dyn(pw, 3 + s);
            const u32x2 o0 = __builtin_bit_cast(u32x2, __builtin_amdgcn_ds_read_tr16_b64_v4i16(LDSP(s16x4, str)));
            const u32x2 o1 = __builtin_bit_cast(u32x2, __builtin_amdgcn_ds_read_tr16_b64_v4i16(LDSP(s16x4, str + 4 * KS_BYTES)));
            *(u32x4*)op = (u32x4){o0[0], o0[1], o1[0], o1[1]};
            str += 512; op += 256 * DO;
        }
    }
    asm volatile("Ls4d_end:");
}

extern "C" void kernel_launch(void* const* d_in, const int* in_sizes, int n_in, void* d_out, int out_size, void* d_ws,
                              size_t ws_size, hipStream_t stream) {
    const uint16_t* const* in = (const uint16_t* const*)d_in;
    s4d_conv_kernel<<<160, 512, 0, stream>>>(in[0], in[1], in[2], in[3], in[4], in[5], in[6], in[7], in[8], in[9], in[10],
                                             in[11], in[12], in[13], (uint16_t*)d_out
        );
}
```
